```python
import torch

def _softmax(x, axis=-1):
    return torch.softmax(x, dim=axis)
def _gelu(x):
    return torch.nn.functional.gelu(x, approximate="tanh")
def _silu(x):
    return torch.nn.functional.silu(x)
def _sigmoid(x):
    return torch.sigmoid(x)
def _layernorm(x, axis=-1, eps=1e-5):
    m = x.mean(dim=axis, keepdim=True)
    v = x.var(dim=axis, keepdim=True, unbiased=False)
    return (x - m) / torch.sqrt(v + eps)
def _scatter_add(tgt, idx, src, axis=0):
    return tgt.clone().index_add_(axis, idx, src)
def _segment_sum(src, seg, n):
    out = torch.zeros((n,) + src.shape[1:], dtype=src.dtype)
    return out.index_add_(0, seg, src)
def _pad(x, pw):
    flat = tuple(v for pair in reversed(pw) for v in pair)
    return torch.nn.functional.pad(x, flat)

kernel_name = 'gated_mix__sliding_local_dilated__glu_gated_glu__prompt'

def setup_inputs():

    _w = (lambda a: (lambda t: t.to(torch.bfloat16) if t.is_floating_point() else t)(torch.as_tensor(a)))
    def _A_setup():
        B, S, D = 1, 2048, 448
        return {
            "x": _w((lambda *s: torch.randn(*s, dtype=torch.bfloat16) * 0.05)(B, S, D)),
            "wqkv": _w((lambda *s: torch.randn(*s, dtype=torch.bfloat16) * 0.8)(D, 3 * D)),
            "wo": _w((lambda *s: torch.randn(*s, dtype=torch.bfloat16) * 0.05)(D, D)),
        }
    _A_in = _A_setup()

    def _B_setup():
        B, S, D, F = 1, 2048, 448, 1792
        return {
            "x": _w((lambda *s: torch.randn(*s, dtype=torch.bfloat16) * 0.05)(B, S, D)),
            "w_gate": _w((lambda *s: torch.randn(*s, dtype=torch.bfloat16) * 1.6)(D, F)),
            "w_up": _w((lambda *s: torch.randn(*s, dtype=torch.bfloat16) * 0.1)(D, F)),
            "w_down": _w((lambda *s: torch.randn(*s, dtype=torch.bfloat16) * 0.1)(F, D)),
        }
    _B_in = _B_setup()

    return {
        "A_x": _A_in["x"],
        "A_wqkv": _A_in["wqkv"],
        "A_wo": _A_in["wo"],
        "B_x": _B_in["x"],
        "B_w_gate": _B_in["w_gate"],
        "B_w_up": _B_in["w_up"],
        "B_w_down": _B_in["w_down"],
        "gate_w": _w((lambda *s: torch.randn(*s, dtype=torch.bfloat16) * 0.05)(448) * 16.0),
    }

def reference(A_x, A_wqkv, A_wo, B_x, B_w_gate, B_w_up, B_w_down, gate_w):
    def _A_ref(x, wqkv, wo):
        B, S, D = x.shape
        H = 16; Dh = D // H
        W, dil = 8, 2
        qkv = (lambda x, *d: x.permute(*d))((x @ wqkv).reshape(B, S, 3, H, Dh), 2, 0, 3, 1, 4)
        q, k, v = qkv[0], qkv[1], qkv[2]
        scale = Dh ** -0.5
        pos = torch.arange(S)
        off = torch.arange(W) * dil
        idx = pos[:, None] - off[None, :]
        mask = idx >= 0
        idx_c = torch.where(mask, idx, 0).reshape(-1)
        kg = k[:, :, idx_c, :].reshape(B, H, S, W, Dh)
        vg = v[:, :, idx_c, :].reshape(B, H, S, W, Dh)
        logits = torch.einsum("bhsd,bhswd->bhsw", q, kg) * scale
        logits = torch.where(mask[None, None, :, :], logits, -1e9)
        attn = _softmax(logits, axis=-1)
        o = torch.einsum("bhsw,bhswd->bhsd", attn, vg)
        return (lambda x, *d: x.permute(*d))(o, 0, 2, 1, 3).reshape(B, S, D) @ wo

    def _B_ref(x, w_gate, w_up, w_down):
        g = torch.einsum("bsd,df->bsf", x, w_gate)
        u = torch.einsum("bsd,df->bsf", x, w_up)
        h = _sigmoid(g) * u
        return torch.einsum("bsf,fd->bsd", h, w_down)

    x = A_x
    g = _sigmoid((x * gate_w).sum(-1, keepdims=True))
    a = _A_ref(x, A_wqkv, A_wo)
    b = _B_ref(x, B_w_gate, B_w_up, B_w_down)
    return g * a + (1 - g) * b
```

```cpp
#include <hip/hip_runtime.h>
#include <hip/hip_bf16.h>
typedef __hip_bfloat16 bf16;
constexpr int S=2048, D=448, F=1792, H=16, DH=28;

__global__ void gemm_naive(const bf16* A, const bf16* B, float* C, int M, int N, int K){
  int n = blockIdx.x*blockDim.x+threadIdx.x; int m = blockIdx.y;
  if(n>=N) return;
  float acc=0.f;
  for(int k=0;k<K;++k) acc += __bfloat162float(A[(size_t)m*K+k])*__bfloat162float(B[(size_t)k*N+n]);
  C[(size_t)m*N+n]=acc;
}
__global__ void attn_naive(const float* qkv, bf16* o){
  int t = blockIdx.x*blockDim.x+threadIdx.x; if(t>=S*H) return;
  int s=t/H, h=t%H;
  float lg[8]; float mx=-1e30f;
  const float* q = qkv + (size_t)s*3*D + h*DH;
  for(int w=0;w<8;++w){ int p=s-2*w; if(p<0){lg[w]=-1e9f;} else { const float* k=qkv+(size_t)p*3*D+D+h*DH; float a=0; for(int d=0;d<DH;++d)a+=q[d]*k[d]; lg[w]=a*rsqrtf((float)DH);} mx=fmaxf(mx,lg[w]); }
  float sum=0; for(int w=0;w<8;++w){lg[w]=__expf(lg[w]-mx); sum+=lg[w];}
  for(int d=0;d<DH;++d){ float a=0; for(int w=0;w<8;++w){int p=s-2*w; if(p<0)p=0; a+=lg[w]*qkv[(size_t)p*3*D+2*D+h*DH+d];} o[(size_t)s*D+h*DH+d]=__float2bfloat16(a/sum);}
}
__global__ void glu_naive(const float* g,const float* u,bf16* h){
  int t=blockIdx.x*blockDim.x+threadIdx.x; if(t>=S*F)return;
  h[t]=__float2bfloat16(u[t]/(1.f+__expf(-g[t])));
}
__global__ void mix_naive(const bf16* x,const bf16* gw,const float* a,const float* b,bf16* out){
  int s=blockIdx.x; __shared__ float red[64];
  float acc=0; for(int d=threadIdx.x;d<D;d+=64) acc+=__bfloat162float(x[s*D+d])*__bfloat162float(gw[d]);
  red[threadIdx.x]=acc; __syncthreads();
  float tot=0; for(int i=0;i<64;++i) tot+=red[i];
  float g=1.f/(1.f+__expf(-tot));
  for(int d=threadIdx.x;d<D;d+=64) out[s*D+d]=__float2bfloat16(g*a[s*D+d]+(1.f-g)*b[s*D+d]);
}
extern "C" void kernel_launch(void* const* d_in, const int* in_sizes, int n_in, void* d_out, int out_size, void* d_ws, size_t ws_size, hipStream_t stream){
  const bf16* x=(const bf16*)d_in[0]; const bf16* wqkv=(const bf16*)d_in[1]; const bf16* wo=(const bf16*)d_in[2];
  const bf16* wg=(const bf16*)d_in[4]; const bf16* wu=(const bf16*)d_in[5]; const bf16* wd=(const bf16*)d_in[6]; const bf16* gw=(const bf16*)d_in[7];
  char* ws=(char*)d_ws;
  float* qkv=(float*)ws; ws+=(size_t)S*3*D*4;
  float* g=(float*)ws; ws+=(size_t)S*F*4;
  float* u=(float*)ws; ws+=(size_t)S*F*4;
  float* a=(float*)ws; ws+=(size_t)S*D*4;
  float* b=(float*)ws; ws+=(size_t)S*D*4;
  bf16* o=(bf16*)ws; ws+=(size_t)S*D*2;
  bf16* h=(bf16*)ws; ws+=(size_t)S*F*2;
  gemm_naive<<<dim3((3*D+63)/64,S),64,0,stream>>>(x,wqkv,qkv,S,3*D,D);
  gemm_naive<<<dim3((F+63)/64,S),64,0,stream>>>(x,wg,g,S,F,D);
  gemm_naive<<<dim3((F+63)/64,S),64,0,stream>>>(x,wu,u,S,F,D);
  attn_naive<<<(S*H+63)/64,64,0,stream>>>(qkv,o);
  glu_naive<<<(S*F+255)/256,256,0,stream>>>(g,u,h);
  gemm_naive<<<dim3((D+63)/64,S),64,0,stream>>>(o,wo,a,S,D,D);
  gemm_naive<<<dim3((D+63)/64,S),64,0,stream>>>(h,wd,b,S,D,F);
  mix_naive<<<S,64,0,stream>>>(x,gw,a,b,(bf16*)d_out);
}
```

```cpp
#include <hip/hip_runtime.h>
#include <hip/hip_bf16.h>
#include <type_traits>

#define LAS __attribute__((address_space(3)))
#define DI __device__ __forceinline__
typedef short s16x4 __attribute__((ext_vector_type(4)));
typedef short s16x8 __attribute__((ext_vector_type(8)));
typedef __bf16 bf16x8 __attribute__((ext_vector_type(8)));
typedef __bf16 bf16x2 __attribute__((ext_vector_type(2)));
typedef float f32x4 __attribute__((ext_vector_type(4)));
typedef float f32x2 __attribute__((ext_vector_type(2)));
typedef unsigned v4u __attribute__((ext_vector_type(4)));
typedef unsigned v2u __attribute__((ext_vector_type(2)));
typedef __amdgpu_buffer_rsrc_t rsrc_t;

constexpr int S_ = 2048, D_ = 448, F_ = 1792;
constexpr unsigned XS = 896, QS = 2688, GS = 3584;
constexpr unsigned A2_OFF = 0, A2_RT = 72 * 1024;
constexpr unsigned QK_OFF = A2_OFF + 128 * A2_RT, QK_RP = 224 * 256;
constexpr unsigned G_OFF = QK_OFF + 64 * QK_RP;
constexpr unsigned ACK_OFF = G_OFF + 8192;
constexpr unsigned REQ_OFF = ACK_OFF + 256 * 128;
constexpr unsigned WS_BYTES = REQ_OFF + 256 * 256;
constexpr unsigned STAMP_OFF = WS_BYTES;
constexpr unsigned OOB = 0x80000000u;
constexpr unsigned KX0 = 0x9E3779B9u, KX1 = 0x7F4A7C15u;
constexpr unsigned SPIN_CAP = 1u << 17;
constexpr int SC1 = 16;

template <int I, int N, class Fn> DI void sfor(Fn&& f) {
  if constexpr (I < N) { f(std::integral_constant<int, I>{}); sfor<I + 1, N>(f); }
}
DI unsigned opaque(unsigned v) { asm("" : "+v"(v)); return v; }
DI void dma(unsigned m0, unsigned voff, rsrc_t rs, unsigned soff) {
  asm volatile("s_mov_b32 m0, %0\n\ts_nop 0\n\tbuffer_load_dwordx4 %1, %2, %3 offen lds" ::"s"(m0), "v"(voff), "s"(rs), "s"(soff) : "memory");
}
DI void dma_lo16(unsigned m0, unsigned voff, rsrc_t rs, unsigned soff) {
  asm volatile("s_mov_b32 m0, %0\n\ts_mov_b64 exec, 0xffff\n\tbuffer_load_dwordx4 %1, %2, %3 offen lds\n\ts_mov_b64 exec, -1"
               ::"s"(m0), "v"(voff), "s"(rs), "s"(soff) : "memory");
}
template <int N> DI void sync() { asm volatile("s_waitcnt vmcnt(%0) lgkmcnt(0)\n\ts_barrier" ::"i"(N) : "memory"); }
DI v4u ld128(unsigned voff, rsrc_t rs, unsigned soff) {
  v4u r; asm volatile("buffer_load_dwordx4 %0, %1, %2, %3 offen sc1" : "=v"(r) : "v"(voff), "s"(rs), "s"(soff) : "memory"); return r;
}
DI v2u ld64(unsigned voff, rsrc_t rs) {
  v2u r; asm volatile("buffer_load_dwordx2 %0, %1, %2, 0 offen sc1" : "=v"(r) : "v"(voff), "s"(rs) : "memory"); return r;
}
DI unsigned ld32(unsigned voff, rsrc_t rs, unsigned soff) {
  unsigned r; asm volatile("buffer_load_dword %0, %1, %2, %3 offen sc1" : "=v"(r) : "v"(voff), "s"(rs), "s"(soff) : "memory"); return r;
}
DI v2u ld64_now(unsigned voff, rsrc_t rs) {
  v2u r; asm volatile("buffer_load_dwordx2 %0, %1, %2, 0 offen sc1\n\ts_waitcnt vmcnt(0)" : "=&v"(r) : "v"(voff), "s"(rs) : "memory"); return r;
}
template <bool S1> DI void st128(v4u v, unsigned voff, rsrc_t rs, unsigned soff) {
  if constexpr (S1) asm volatile("buffer_store_dwordx4 %0, %1, %2, %3 offen sc1\n\ts_nop 1" ::"v"(v), "v"(voff), "s"(rs), "s"(soff) : "memory");
  else asm volatile("buffer_store_dwordx4 %0, %1, %2, %3 offen\n\ts_nop 1" ::"v"(v), "v"(voff), "s"(rs), "s"(soff) : "memory");
}
template <int N, class T> DI void wait_for(T& a) { asm volatile("s_waitcnt vmcnt(%1)" : "+v"(a) : "i"(N) : "memory"); }
template <class T> DI void landed(T& a) { asm volatile("" : "+v"(a)::"memory"); }

template <int OFF> DI s16x4 tr(const unsigned (&b)[3]) {
  return __builtin_amdgcn_ds_read_tr16_b64_v4i16((LAS s16x4*)(size_t)(b[OFF >> 16] + (OFF & 0xFFFF)));
}
template <int OFF, int STEP> DI s16x8 tr2(const unsigned (&b)[3]) {
  return __builtin_shufflevector(tr<OFF>(b), tr<OFF + STEP>(b), 0, 1, 2, 3, 4, 5, 6, 7);
}
template <int OFF> DI s16x8 rd128(const unsigned (&b)[3]) { return *(const LAS s16x8*)(size_t)(b[OFF >> 16] + (OFF & 0xFFFF)); }
DI void seg3(unsigned (&b)[3], unsigned v) { b[0] = opaque(v); b[1] = opaque(v + 65536u); b[2] = opaque(v + 131072u); }
DI f32x4 mfma32(s16x8 a, s16x8 b, f32x4 c) {
  return __builtin_amdgcn_mfma_f32_16x16x32_bf16(__builtin_bit_cast(bf16x8, a), __builtin_bit_cast(bf16x8, b), c, 0, 0, 0);
}
DI f32x4 mfma16(v2u a, v2u b, f32x4 c) {
  return __builtin_amdgcn_mfma_f32_16x16x16bf16_1k(__builtin_bit_cast(s16x4, a), __builtin_bit_cast(s16x4, b), c, 0, 0, 0);
}
DI unsigned pk(float a, float b) { f32x2 p = {a, b}; return __builtin_bit_cast(unsigned, __builtin_convertvector(p, bf16x2)); }
DI float glu(float g, float u) { return u * __builtin_amdgcn_rcpf(1.0f + __builtin_amdgcn_exp2f(g * -1.4426950408889634f)); }
template <bool MAX> DI float red4(float v) {
  unsigned a = __float_as_uint(v);
  auto t = __builtin_amdgcn_permlane16_swap(a, a, false, false);
  unsigned t0 = t[0], t1 = t[1];
  a = __float_as_uint(MAX ? fmaxf(__uint_as_float(t0), __uint_as_float(t1)) : __uint_as_float(t0) + __uint_as_float(t1));
  t = __builtin_amdgcn_permlane32_swap(a, a, false, false);
  t0 = t[0]; t1 = t[1];
  return MAX ? fmaxf(__uint_as_float(t0), __uint_as_float(t1)) : __uint_as_float(t0) + __uint_as_float(t1);
}
DI int mod7(int v) { return v >= 7 ? v - 7 : v; }
DI bool all_lanes(bool ok) { return __builtin_amdgcn_ballot_w64(!ok) == 0ull; }

DI bool ack_ok(v2u v, v2u a0, unsigned need) { const unsigned d = v.x - a0.x; return all_lanes(need == 0u || (v.y == a0.y && d >= need && d <= 2u)); }
DI void ack_wait(v2u first, v2u a0, unsigned need, unsigned voff, rsrc_t rs) {
  if (ack_ok(first, a0, need)) return;
  for (unsigned spins = 0; spins < SPIN_CAP; ++spins) {
    const v2u v = ld64_now(voff, rs);
    if (ack_ok(v, a0, need)) return;
    __builtin_amdgcn_s_sleep(2);
  }
}
DI bool req_ok(v2u rq, v2u ak, bool valid) { return all_lanes(!valid || (rq.x == (ak.x ^ KX0) && rq.y == (ak.y ^ KX1))); }
DI void req_wait(v2u first, v2u ak, bool valid, unsigned voff, rsrc_t rs) {
  if (req_ok(first, ak, valid)) return;
  for (unsigned spins = 0; spins < SPIN_CAP; ++spins) {
    const v2u v = ld64_now(voff, rs);
    if (req_ok(v, ak, valid)) return;
    __builtin_amdgcn_s_sleep(2);
  }
}

#define STAMP_DECL
#define ST(i)
#define STAMP_OUT

struct Params {
  const char *x, *wqkv, *wo, *wg, *wu, *wd, *gw;
  char *out, *ws;
};

#define ROTN 7

#define SCHED 0
constexpr int PRO = SCHED == 0 ? 10 : SCHED == 1 ? 15 : 20;
constexpr int need_unit(int k) { return k < 7 ? 5 * k + 4 : k < 14 ? 36 + 2 * (k - 7) : 52 + 4 * (k - 14); }
constexpr int freed(int k) { return k == 0 ? 0 : need_unit(k - 1) + 1; }
constexpr int WANT[23] = {15, 20, 25, 30, 35, 40, 45, 49, 53, 57, 60, 63, 65, 67, 69, 73, 77, 81, 85, 85, 85, 85, 85};
constexpr int issued(int k) { return k < 0 ? PRO : (WANT[k] < freed(k) + 20 ? WANT[k] : freed(k) + 20); }
constexpr int half(int k) { return issued(k - 1) + (issued(k) - issued(k - 1) + 1) / 2; }
constexpr int LISS[23] = {0, 0, 0, 0, 0, 0, 0, 0, 0, 0, 0, 0, 0, 8, 12, 14, 20, 24, 28, 32, 36, 36, 36};
constexpr int liss(int k) { return k < 0 ? 0 : LISS[k]; }
constexpr int unit_batch(int q) { int k = -1; while (issued(k) <= q) ++k; return k; }
constexpr int load_batch(int l) { int k = 0; while (liss(k) <= l) ++k; return k; }
constexpr int after_unit(int q, int upto) { return (issued(upto) - 1 - q) + (liss(upto) - liss(unit_batch(q) - 1)); }
constexpr int after_load(int l, int upto) { return (liss(upto) - 1 - l) + (issued(upto) - issued(load_batch(l))); }
constexpr int ops_in(int from, int upto) { return (issued(upto) - issued(from - 1)) + (liss(upto) - liss(from - 1)); }

__global__ __launch_bounds__(512, 2) void fused_kernel(Params p) {
  __shared__ __attribute__((aligned(16))) char smem[163840];
  const unsigned lds0 = (unsigned)(size_t)(LAS char*)smem;
  const int tid = threadIdx.x, lane = tid & 63, w = __builtin_amdgcn_readfirstlane(tid >> 6);
  const int grp = lane >> 4, m16 = lane & 15, q4 = m16 >> 2, p4 = lane & 3, l8 = lane >> 3, c8 = lane & 7;
  const int b = blockIdx.x, xcd = b & 7, bi = b >> 3;
  STAMP_DECL;
  ST(0);
  const rsrc_t rx = __builtin_amdgcn_make_buffer_rsrc((void*)p.x, (short)0, S_ * D_ * 2, 0x00020000);
  const rsrc_t rq = __builtin_amdgcn_make_buffer_rsrc((void*)p.wqkv, (short)0, D_ * 3 * D_ * 2, 0x00020000);
  const rsrc_t rw = __builtin_amdgcn_make_buffer_rsrc((void*)p.ws, (short)0, WS_BYTES, 0x00020000);
  const unsigned m0w = lds0 + w * 1024;
  const unsigned vx = (8 * w + l8) * XS + ((c8 ^ (4 * (w & 1) + grp)) << 4);
  const unsigned fsw = (c8 ^ (((grp & 1) | ((w & 1) << 1)) << 1)) << 4;
  const unsigned vq = (8 * w + l8) * QS + fsw;
  const unsigned f1 = (q4 >> 1) | ((grp & 1) << 1);

  if (bi < 28) {
    const int lin = (xcd >> 1) * 28 + bi, s = lin >> 3, r = (xcd & 1) * 8 + (lin & 7);
    const int t2 = bi >= 14 ? bi - 14 : bi, r2 = 2 * xcd + (bi >= 14 ? 1 : 0);
    const int n2 = t2 >= 7 ? t2 - 7 : t2, rb2 = 2 * r2 + (t2 >= 7 ? 1 : 0);
    const rsrc_t rg_ = __builtin_amdgcn_make_buffer_rsrc((void*)p.wg, (short)0, D_ * F_ * 2, 0x00020000);
    const rsrc_t ru = __builtin_amdgcn_make_buffer_rsrc((void*)p.wu, (short)0, D_ * F_ * 2, 0x00020000);
    const rsrc_t rd = __builtin_amdgcn_make_buffer_rsrc((void*)p.wd, (short)0, F_ * D_ * 2, 0x00020000);
    const rsrc_t ro = __builtin_amdgcn_make_buffer_rsrc((void*)p.wo, (short)0, D_ * D_ * 2, 0x00020000);
    const rsrc_t rout = __builtin_amdgcn_make_buffer_rsrc((void*)p.out, (short)0, S_ * D_ * 2, 0x00020000);
    const int c0 = (((r & 7) + 2 * s) % ROTN) * (7 / ROTN);
    const int rot = (2 * n2 + 3 * (rb2 & 3)) % 14;
    const unsigned vg = (8 * w + l8) * GS + fsw;
    const unsigned vw2 = l8 * XS + ((c8 ^ (grp << 1)) << 4);
    const int rg = w & 3, ch = w >> 2;

    const unsigned ackc = lane < 16 ? ACK_OFF + (r2 * 14 + t2) * 128 + lane * 8 : OOB;
    v2u a0 = ld64(ackc, rw);

    auto p2unit = [&](auto pc) -> int {
      constexpr int P = decltype(pc)::value;
      if constexpr (P < 28) { int v = (P % 14) + rot; v = v >= 14 ? v - 14 : v; return 2 * v + P / 14; }
      else return 28 + ((P - 28 + rot) & 7);
    };
    auto issue_unit = [&](auto qc) {
      constexpr int Q = decltype(qc)::value;
      const unsigned m0 = m0w + (Q % 20) * 8192;
      if constexpr (Q < 35) {
        constexpr int i = Q / 5, u = Q % 5;
        const int c = mod7(i + c0);
        if constexpr (u < 2) dma(m0, vx, rx, (r * 128 + 64 * u) * XS + c * 128);
        else if constexpr (u == 2) dma(m0, vq, rq, c * 64 * QS + s * 128);
        else dma(m0, vg, u == 3 ? rg_ : ru, c * 64 * GS + s * 256);
      } else if constexpr (Q < 49) {
        constexpr int j = (Q - 35) / 2, u = (Q - 35) % 2;
        const int c = mod7(j + c0);
        dma(m0, vg, u == 0 ? rg_ : ru, c * 64 * GS + s * 256 + 128);
      } else {
        constexpr int P = Q - 49;
        const int v = p2unit(std::integral_constant<int, P>{});
        if constexpr (P < 28) dma(m0, vw2, rd, (64 * v + 8 * w) * XS + n2 * 128);
        else dma(m0, vw2, ro, (28 * (2 * (v - 28) + ch) + 8 * rg) * XS + n2 * 128);
      }
    };
    v4u al[36];
    const unsigned va2 = lane * 16;
    auto issue_load = [&](auto lc) {
      constexpr int L = decltype(lc)::value;
      const int v = p2unit(lc);
      al[L] = ld128(va2, rw, A2_OFF + (rb2 * 4 + rg) * A2_RT + (2 * v + ch) * 1024);
    };
    auto batch = [&](auto kc) {
      constexpr int K = decltype(kc)::value;
      sfor<issued(K - 1), issued(K)>(issue_unit);
      sfor<liss(K - 1), liss(K)>(issue_load);
    };
    sfor<0, PRO>(issue_unit);

    unsigned ackp, reqp; bool pvalid;
    {
      const int u = s >= 7 ? s - 7 : s, isk = s >= 7, a = lane & 1, nxt = lane >= 16;
      const bool inr = u >= 3 * a && u <= 3 * a + 3;
      pvalid = lane < 14 || (lane < 16 && inr) || (lane < 18 && inr && isk && r < 15);
      const unsigned o1 = ACK_OFF + (r * 14 + lane) * 128 + s * 8;
      const unsigned o2 = ACK_OFF + (224 + 2 * (r + nxt) + a) * 128 + ((nxt ? 8 : isk ? 4 : 0) + u - 3 * a) * 8;
      ackp = pvalid ? (lane < 14 ? o1 : o2) : OOB;
      reqp = pvalid ? REQ_OFF + (r * 16 + s) * 256 + lane * 8 : OOB;
    }
    unsigned bw0[3], bw1[3], bx0[3], bx1[3];
    {
      const unsigned rowp = lds0 + (8 * grp + q4) * 128 + p4 * 8;
      seg3(bw0, rowp + (((2 * ch) ^ f1) << 5));
      seg3(bw1, rowp + (((2 * ch + 1) ^ f1) << 5));
      const unsigned xr0 = lds0 + (32 * rg + m16) * 128;
      seg3(bx0, xr0 + ((grp ^ (m16 >> 1)) << 4));
      seg3(bx1, xr0 + (((4 + grp) ^ (m16 >> 1)) << 4));
    }
    s16x8 xr[14][2], wf[2][6];
    f32x4 acc1[6][2], acc2[4][2];
    sfor<0, 6>([&](auto i) { acc1[i][0] = f32x4{0, 0, 0, 0}; acc1[i][1] = f32x4{0, 0, 0, 0}; });
    sfor<0, 4>([&](auto i) { acc2[i][0] = f32x4{0, 0, 0, 0}; acc2[i][1] = f32x4{0, 0, 0, 0}; });
    auto rd1 = [&](auto sc, auto jc) {
      constexpr int T = decltype(sc)::value, J = decltype(jc)::value, kk = T & 1;
      if constexpr (T < 14) {
        constexpr int q0 = 5 * (T / 2);
        if constexpr (J < 2) {
          constexpr int xo = (q0 % 20) * 8192 + J * 2048;
          xr[T][J] = kk ? rd128<xo>(bx1) : rd128<xo>(bx0);
        } else {
          constexpr int o = ((q0 + 2 + (J - 2) / 2) % 20) * 8192 + kk * 4096;
          wf[kk][J - 2] = (J & 1) ? tr2<o, 512>(bw1) : tr2<o, 512>(bw0);
        }
      } else if constexpr (T < 28) {
        constexpr int o = ((35 + 2 * ((T - 14) / 2) + J / 2) % 20) * 8192 + kk * 4096;
        wf[kk][J] = (J & 1) ? tr2<o, 512>(bw1) : tr2<o, 512>(bw0);
      }
    };
    auto mm1 = [&](auto sc, auto c) {
      constexpr int T = decltype(sc)::value, kk = T & 1;
      if constexpr (T < 14) { acc1[c][0] = mfma32(wf[kk][c], xr[T][0], acc1[c][0]); acc1[c][1] = mfma32(wf[kk][c], xr[T][1], acc1[c][1]); }
      else { acc2[c][0] = mfma32(wf[kk][c], xr[T - 14][0], acc2[c][0]); acc2[c][1] = mfma32(wf[kk][c], xr[T - 14][1], acc2[c][1]); }
    };
    auto step1 = [&](auto sc, auto q0c, auto q1c) {
      constexpr int T = decltype(sc)::value, Q0 = decltype(q0c)::value, Q1 = decltype(q1c)::value;
      constexpr int NM = T < 14 ? 6 : 4, NR = T + 1 < 14 ? 8 : T + 1 < 28 ? 4 : 0;
      sfor<0, NM>([&](auto c) {
        constexpr int C = decltype(c)::value;
        mm1(sc, c);
        __builtin_amdgcn_sched_barrier(0);
        sfor<(2 * C < NR ? 2 * C : NR), (2 * C + 2 < NR ? 2 * C + 2 : NR)>([&](auto j) { rd1(std::integral_constant<int, T + 1>{}, j); });
        __builtin_amdgcn_sched_barrier(0);
        constexpr int first = NM - (Q1 - Q0) > 0 ? NM - (Q1 - Q0) : 0;
        if constexpr (C >= first) {
          constexpr int lo = Q0 + (C - first), hi = C == NM - 1 ? Q1 : lo + 1;
          sfor<lo, hi>(issue_unit);
          __builtin_amdgcn_sched_barrier(0);
        }
      });
    };
    auto store_h = [&](const f32x4& g0, const f32x4& g1, const f32x4& u0, const f32x4& u1, int e, int pair) {
      v4u o;
      o.x = pk(glu(g0[0], u0[0]), glu(g0[1], u0[1])); o.y = pk(glu(g0[2], u0[2]), glu(g0[3], u0[3]));
      o.z = pk(glu(g1[0], u1[0]), glu(g1[1], u1[1])); o.w = pk(glu(g1[2], u1[2]), glu(g1[3], u1[3]));
      st128<true>(o, va2, rw, A2_OFF + (r * 8 + 2 * rg + e) * A2_RT + (4 * s + 2 * pair + ch) * 1024);
    };
    const unsigned pubo = w == 0 ? ackp : OOB;
    v2u prq, pak, poll;

    ST(24);
    sync<after_unit(need_unit(0), -1)>();
    ST(1);

    landed(a0);
    __builtin_amdgcn_raw_buffer_store_b64(v2u{a0.x ^ KX0, a0.y ^ KX1}, rw, (w == 0 && lane < 16) ? REQ_OFF + (r2 * 16 + lane) * 256 + t2 * 8 : OOB, 0, SC1);
    sfor<0, 8>([&](auto j) { rd1(std::integral_constant<int, 0>{}, j); });
    sfor<issued(-1), half(0)>(issue_unit);
    __builtin_amdgcn_sched_barrier(0);
    sfor<0, 14>([&](auto ic) {
      constexpr int I = decltype(ic)::value, K = I + 1;
      step1(std::integral_constant<int, 2 * I>{}, std::integral_constant<int, half(I)>{}, std::integral_constant<int, issued(I)>{});
      sfor<liss(I - 1), liss(I)>(issue_load);
      if constexpr (I == 7) {
        sfor<0, 2>([&](auto t) {
          v4u o = {pk(acc1[t][0][0], acc1[t][0][1]), pk(acc1[t][0][2], acc1[t][0][3]), pk(acc1[t][1][0], acc1[t][1][1]), pk(acc1[t][1][2], acc1[t][1][3])};
          st128<true>(o, va2, rw, QK_OFF + (r * 4 + rg) * QK_RP + (16 * s + 8 * ch + 4 * t) * 256);
        });
        store_h(acc1[2][0], acc1[3][0], acc1[4][0], acc1[5][0], 0, 0);
        store_h(acc1[2][1], acc1[3][1], acc1[4][1], acc1[5][1], 1, 0);

      }
      if constexpr (K < 14) {
        constexpr int nv = after_unit(need_unit(K), K - 1);
        constexpr int n = (K == 10 && ops_in(8, 9) < nv) ? ops_in(8, 9) : (K == 6 && ops_in(4, 5) < nv) ? ops_in(4, 5) : (K == 13 && ops_in(12, 12) < nv) ? ops_in(12, 12) : nv;
        sync<n>();
        ST(1 + K);
        if constexpr (K == 4) { prq = ld64(reqp, rw); pak = ld64(ackp, rw); }
        if constexpr (K == 6) { landed(prq); landed(pak); req_wait(prq, pak, pvalid, reqp, rw); }
        if constexpr (K == 10) { __builtin_amdgcn_raw_buffer_store_b64(v2u{pak.x + 1u, pak.y}, rw, pubo, 0, SC1); }
        if constexpr (K == 12) poll = ld64(ackc, rw);
        if constexpr (K == 13) { landed(poll); ack_wait(poll, a0, lane < 14 ? 1u : 0u, ackc, rw); ST(25); }
        __builtin_amdgcn_sched_barrier(0);
        step1(std::integral_constant<int, 2 * I + 1>{}, std::integral_constant<int, issued(K - 1)>{}, std::integral_constant<int, half(K)>{});
      } else {
        step1(std::integral_constant<int, 2 * I + 1>{}, std::integral_constant<int, 0>{}, std::integral_constant<int, 0>{});
      }
    });

    store_h(acc2[0][0], acc2[1][0], acc2[2][0], acc2[3][0], 0, 1);
    store_h(acc2[0][1], acc2[1][1], acc2[2][1], acc2[3][1], 1, 1);

    unsigned b2[4][3];
    {
      const unsigned f2 = ((grp & 1) << 1) | (q4 >> 1);
      const unsigned rowp = lds0 + (32 * ch + 4 * grp + q4) * 128 + p4 * 8;
      sfor<0, 4>([&](auto t) { seg3(b2[t], rowp + ((t ^ f2) << 5)); });
    }
    f32x4 ah[4], ao[4];
    sfor<0, 4>([&](auto t) { ah[t] = f32x4{0, 0, 0, 0}; ao[t] = f32x4{0, 0, 0, 0}; });
    unsigned gbits;
    s16x8 wv[2][4];
    auto rd2 = [&](auto pc, auto t) {
      constexpr int P = decltype(pc)::value;
      if constexpr (P < 36) wv[P & 1][t] = tr2<((49 + P) % 20) * 8192, 2048>(b2[t]);
    };
    auto sync2 = [&](auto kc) {
      constexpr int K = decltype(kc)::value;
      constexpr int nv = after_unit(need_unit(K), K - 1);
      constexpr int n = (K == 15 || K == 16 || K == 19) && ops_in(K - 1, K - 1) < nv ? ops_in(K - 1, K - 1) : nv;
      sync<n>();
      ST(1 + K);
      if constexpr (K == 15) { __builtin_amdgcn_raw_buffer_store_b64(v2u{pak.x + 2u, pak.y}, rw, lane < 14 ? pubo : OOB, 0, SC1); poll = ld64(ackc, rw); }
      if constexpr (K == 16) { landed(poll); ack_wait(poll, a0, lane < 14 ? 2u : 0u, ackc, rw); ST(26); }
      if constexpr (K == 18) poll = ld64(ackc, rw);
      if constexpr (K == 19) {
        landed(poll); ack_wait(poll, a0, (lane == 14 || lane == 15) ? 1u : 0u, ackc, rw); ST(27);
        gbits = ld32(m16 * 4, rw, G_OFF + (rb2 * 64 + rg * 16) * 4);
      }
      __builtin_amdgcn_sched_barrier(0);
    };
    sync2(std::integral_constant<int, 14>{});
    sfor<0, 4>([&](auto t) { rd2(std::integral_constant<int, 0>{}, t); });
    batch(std::integral_constant<int, 14>{});
    sfor<0, 36>([&](auto pc) {
      constexpr int P = decltype(pc)::value;
      constexpr bool edge = (P & 3) == 3 && P < 35;
      constexpr int K = 14 + P / 4;
      if constexpr (edge) sync2(std::integral_constant<int, K + 1>{});
      wait_for<after_load(P, K)>(al[P]);
      const s16x8 av = __builtin_bit_cast(s16x8, al[P]);
      sfor<0, 4>([&](auto t) {
        if constexpr (P < 28) ah[t] = mfma32(wv[P & 1][t], av, ah[t]); else ao[t] = mfma32(wv[P & 1][t], av, ao[t]);
        __builtin_amdgcn_sched_barrier(0);
        rd2(std::integral_constant<int, P + 1>{}, t);
        __builtin_amdgcn_sched_barrier(0);
      });
      if constexpr (edge) { batch(std::integral_constant<int, K + 1>{}); __builtin_amdgcn_sched_barrier(0); }
    });

    ST(28);
    landed(gbits);
    const float g = __builtin_bit_cast(float, gbits), g1 = 1.0f - g;
    f32x4 v[4];
    sfor<0, 4>([&](auto t) { v[t] = ao[t] * g + ah[t] * g1; });
    LAS f32x4* red = (LAS f32x4*)((LAS char*)smem + 8 * 8192);
    red[(w * 2 + 0) * 64 + lane] = ch ? v[0] : v[2];
    red[(w * 2 + 1) * 64 + lane] = ch ? v[1] : v[3];
    asm volatile("s_waitcnt lgkmcnt(0)\n\ts_barrier" ::: "memory");
    const f32x4 e0 = (ch ? v[2] : v[0]) + red[((w ^ 4) * 2 + 0) * 64 + lane];
    const f32x4 e1 = (ch ? v[3] : v[1]) + red[((w ^ 4) * 2 + 1) * 64 + lane];
    const auto sx = __builtin_amdgcn_permlane16_swap(pk(e0[0], e0[1]), pk(e1[0], e1[1]), false, false);
    const auto sy = __builtin_amdgcn_permlane16_swap(pk(e0[2], e0[3]), pk(e1[2], e1[3]), false, false);
    const v4u o = {sx[0], sy[0], sx[1], sy[1]};
    st128<false>(o, m16 * XS + (grp & 1) * 32 + (grp >> 1) * 16, rout, (rb2 * 64 + rg * 16) * XS + n2 * 128 + ch * 64);
    ST(29);

  } else {
    const int idx = (xcd >> 1) * 4 + (bi - 28), a = idx >> 3, r = (xcd & 1) * 8 + (idx & 7), s = 14 + a;
    const rsrc_t rgw = __builtin_amdgcn_make_buffer_rsrc((void*)p.gw, (short)0, D_ * 2, 0x00020000);
    const int c0 = (((r & 7) + 2 * s) % ROTN) * (7 / ROTN);
    constexpr int SLOT = 51200, GT = 3 * SLOT;
    const bool cvalid = lane < 8 || (lane < 12 && r > 0);
    const unsigned ackc = cvalid ? ACK_OFF + (224 + 2 * r + a) * 128 + lane * 8 : OOB;
    v2u a0 = ld64(ackc, rw);
    const unsigned vh = (2 * w + (l8 & 1)) * XS + ((c8 ^ w) << 4);
    auto issue_stage = [&](auto ic) {
      constexpr int I = decltype(ic)::value;
      const int c = mod7(I + c0);
      const unsigned m0 = m0w + (I % 3) * SLOT;
      dma(m0, vx, rx, (r * 128) * XS + c * 128);
      dma(m0 + 8192, vx, rx, (r * 128 + 64) * XS + c * 128);
      dma_lo16(lds0 + (I % 3) * SLOT + 49152 + w * 256, vh, rx, r > 0 ? (r * 128 - 16) * XS + c * 128 : OOB);
      sfor<0, 4>([&](auto u) { dma(m0 + 16384 + u * 8192, vq, rq, c * 64 * QS + 1792 + (3 * a + u) * 128); });
    };
    dma(lds0 + GT, lane * 16, rgw, 0);
    issue_stage(std::integral_constant<int, 0>{});
    issue_stage(std::integral_constant<int, 1>{});

    const bool pvalid = lane < 14;
    const unsigned ackp = pvalid ? ACK_OFF + (r * 14 + lane) * 128 + s * 8 : OOB;
    const unsigned reqp = pvalid ? REQ_OFF + (r * 16 + s) * 256 + lane * 8 : OOB;
    unsigned reqc;
    {
      const int ps = lane < 4 ? 3 * a + lane : 7 + 3 * a + (lane & 3), pr = lane < 8 ? r : r - 1;
      reqc = (w == 0 && cvalid) ? REQ_OFF + (pr * 16 + ps) * 256 + ((lane < 8 ? 14 : 16) + a) * 8 : OOB;
    }
    unsigned bv0[3], bv1[3], bx0[3], bx1[3], bg0[3], bg1[3];
    {
      const unsigned rowp = lds0 + 16384 + (8 * grp + q4) * 128;
      const int lc0 = 28 * w + 32 * a + 4 * p4, lc1 = 28 * w + 32 * a + 16 + (p4 == 3 ? 8 : 4 * p4);
      seg3(bv0, rowp + (lc0 >> 6) * 8192 + ((((lc0 >> 4) & 3) ^ f1) << 5) + (lc0 & 15) * 2);
      seg3(bv1, rowp + (lc1 >> 6) * 8192 + ((((lc1 >> 4) & 3) ^ f1) << 5) + (lc1 & 15) * 2);
      const unsigned xr0 = lds0 + m16 * 128;
      seg3(bx0, xr0 + ((grp ^ (m16 >> 1)) << 4));
      seg3(bx1, xr0 + (((4 + grp) ^ (m16 >> 1)) << 4));
      seg3(bg0, xr0 + w * 2048 + ((grp ^ (m16 >> 1)) << 4));
      seg3(bg1, xr0 + w * 2048 + (((4 + grp) ^ (m16 >> 1)) << 4));
    }
    s16x8 xf[2][9], xg[2], gf[2], vf[2][2];
    f32x4 av[9][2], ag = {0, 0, 0, 0};
    sfor<0, 9>([&](auto j) { av[j][0] = f32x4{0, 0, 0, 0}; av[j][1] = f32x4{0, 0, 0, 0}; });
    auto rdA = [&](auto sc, auto jc) {
      constexpr int T = decltype(sc)::value, J = decltype(jc)::value, kk = T & 1, so = ((T / 2) % 3) * SLOT;
      if constexpr (T < 14) {
        if constexpr (J < 2) vf[kk][J] = J ? tr2<so + kk * 4096, 512>(bv1) : tr2<so + kk * 4096, 512>(bv0);
        else if constexpr (J == 2) xf[kk][0] = kk ? rd128<so + 49152>(bx1) : rd128<so + 49152>(bx0);
        else if constexpr (J < 11) xf[kk][J - 2] = kk ? rd128<so + (J - 3) * 2048>(bx1) : rd128<so + (J - 3) * 2048>(bx0);
        else if constexpr (J == 11) xg[kk] = kk ? rd128<so>(bg1) : rd128<so>(bg0);
        else gf[kk] = *(const LAS s16x8*)(size_t)(lds0 + GT + mod7(T / 2 + c0) * 128 + kk * 64 + grp * 16);
      }
    };
    auto stepA = [&](auto sc) {
      constexpr int T = decltype(sc)::value, kk = T & 1;
      sfor<0, 9>([&](auto j) {
        constexpr int J = decltype(j)::value;
        av[j][0] = mfma32(xf[kk][j], vf[kk][0], av[j][0]); av[j][1] = mfma32(xf[kk][j], vf[kk][1], av[j][1]);
        __builtin_amdgcn_sched_barrier(0);
        sfor<(2 * J < 13 ? 2 * J : 13), (2 * J + 2 < 13 ? 2 * J + 2 : 13)>([&](auto i) { rdA(std::integral_constant<int, T + 1>{}, i); });
        __builtin_amdgcn_sched_barrier(0);
      });
      ag = mfma32(gf[kk], xg[kk], ag);
      __builtin_amdgcn_sched_barrier(0);
    };
    v2u prq, pak;
    sync<7>();
    ST(1);
    landed(a0);
    __builtin_amdgcn_raw_buffer_store_b64(v2u{a0.x ^ KX0, a0.y ^ KX1}, rw, reqc, 0, SC1);
    sfor<0, 13>([&](auto j) { rdA(std::integral_constant<int, 0>{}, j); });
    issue_stage(std::integral_constant<int, 2>{});
    __builtin_amdgcn_sched_barrier(0);
    sfor<0, 7>([&](auto ic) {
      constexpr int I = decltype(ic)::value, K = I + 1;
      stepA(std::integral_constant<int, 2 * I>{});
      if constexpr (K < 7) {
        sync<(K < 6 ? 7 : 0)>();
        if constexpr (K == 3) { prq = ld64(reqp, rw); pak = ld64(ackp, rw); }
        if constexpr (K == 6) { landed(prq); landed(pak); req_wait(prq, pak, pvalid, reqp, rw); }
        __builtin_amdgcn_sched_barrier(0);
      }
      stepA(std::integral_constant<int, 2 * I + 1>{});
      if constexpr (K + 2 < 7) { issue_stage(std::integral_constant<int, K + 2>{}); __builtin_amdgcn_sched_barrier(0); }
    });
    ST(2);
    v2u vb[9][2];
    sfor<0, 9>([&](auto j) { sfor<0, 2>([&](auto t) { vb[j][t] = v2u{pk(av[j][t][0], av[j][t][1]), pk(av[j][t][2], av[j][t][3])}; }); });
    const float gs = __builtin_amdgcn_rcpf(1.0f + __builtin_amdgcn_exp2f(ag[0] * -1.4426950408889634f));
    __builtin_amdgcn_raw_buffer_store_b32(__builtin_bit_cast(unsigned, gs), rw, (a == 0 && lane < 16) ? lane * 4 : OOB, G_OFF + (r * 128 + w * 16) * 4, SC1);
    ack_wait(ld64_now(ackc, rw), a0, cvalid ? 1u : 0u, ackc, rw);
    ST(3);
    const int hd = 8 * a + w;
    const unsigned vlo = lane * 16, vhi = grp < 3 ? 1024 + lane * 16 : OOB;
    v4u ql[5], qh[5], kl[5], kh[5];
    sfor<0, 5>([&](auto pc) {
      constexpr int P = decltype(pc)::value;
      const unsigned base = QK_OFF + (r * 4 + P - 1) * QK_RP + 7 * hd * 256;
      const unsigned kb = (P == 0 && r == 0) ? OOB : base + 112 * 256;
      kl[P] = ld128(vlo, rw, kb);
      kh[P] = ld128((P == 0 && r == 0) ? OOB : vhi, rw, (P == 0 && r == 0) ? 0u : kb);
      if constexpr (P > 0) { ql[P] = ld128(vlo, rw, base); qh[P] = ld128(vhi, rw, base); }
    });
    const bool odd = lane & 1;
    const bool curA = 4 * grp + (lane & 1) <= m16, curB = 4 * grp + 2 + (lane & 1) <= m16;
    const float sc = 0.18898223650461361f * 1.4426950408889634f;
    sfor<0, 8>([&](auto ic) {
      constexpr int I = decltype(ic)::value, P = 1 + I / 2, e = I & 1, PP = e ? P : P - 1, ep = e ^ 1;
      if constexpr (e == 0) {
        asm volatile("s_waitcnt vmcnt(%6)" : "+v"(kl[P]), "+v"(kh[P]), "+v"(ql[P]), "+v"(qh[P]), "+v"(kl[P - 1]), "+v"(kh[P - 1]) : "i"(4 * (4 - P) + 2 * (P - 1)) : "memory");
      }
      const v2u q0 = {ql[P][2 * e], ql[P][2 * e + 1]}, q1 = {qh[P][2 * e], qh[P][2 * e + 1]};
      f32x4 sp = {0, 0, 0, 0}, scu = {0, 0, 0, 0};
      sp = mfma16(v2u{kl[PP][2 * ep], kl[PP][2 * ep + 1]}, q0, sp); sp = mfma16(v2u{kh[PP][2 * ep], kh[PP][2 * ep + 1]}, q1, sp);
      scu = mfma16(v2u{kl[P][2 * e], kl[P][2 * e + 1]}, q0, scu); scu = mfma16(v2u{kh[P][2 * e], kh[P][2 * e + 1]}, q1, scu);
      float la = curA ? (odd ? scu[1] : scu[0]) : (odd ? sp[1] : sp[0]);
      float lb = curB ? (odd ? scu[3] : scu[2]) : (odd ? sp[3] : sp[2]);
      la *= sc; lb *= sc;
      if constexpr (I == 0) { if (r == 0) { la = curA ? la : -1.4426950408889634e9f; lb = curB ? lb : -1.4426950408889634e9f; } }
      const float mx = red4<true>(fmaxf(la, lb));
      float pa = __builtin_amdgcn_exp2f(la - mx), pb = __builtin_amdgcn_exp2f(lb - mx);
      const float sm = red4<false>(pa + pb);
      const float inv = __builtin_amdgcn_rcpf(sm);
      pa *= inv; pb *= inv;
      const unsigned ta = odd ? pk(0.f, pa) : pk(pa, 0.f), tb = odd ? pk(0.f, pb) : pk(pb, 0.f);
      const v2u pp = {curA ? 0u : ta, curB ? 0u : tb}, pc2 = {curA ? ta : 0u, curB ? tb : 0u};
      f32x4 o0 = {0, 0, 0, 0}, o1 = {0, 0, 0, 0};
      o0 = mfma16(vb[I][0], pp, o0); o0 = mfma16(vb[I + 1][0], pc2, o0);
      o1 = mfma16(vb[I][1], pp, o1); o1 = mfma16(vb[I + 1][1], pc2, o1);
      v4u o = {pk(o0[0], o0[1]), pk(o0[2], o0[3]), grp < 3 ? pk(o1[0], o1[1]) : 0u, grp < 3 ? pk(o1[2], o1[3]) : 0u};
      st128<true>(o, vlo, rw, A2_OFF + (r * 8 + I) * A2_RT + (56 + hd) * 1024);
    });
    ST(4);
    sync<0>();
    __builtin_amdgcn_raw_buffer_store_b64(v2u{pak.x + 1u, pak.y}, rw, w == 0 ? ackp : OOB, 0, SC1);
    ST(5);
  }
  STAMP_OUT;
}

extern "C" void kernel_launch(void* const* d_in, const int* in_sizes, int n_in, void* d_out, int out_size, void* d_ws, size_t ws_size,
                              hipStream_t stream) {
  Params p;
  p.x = (const char*)d_in[0]; p.wqkv = (const char*)d_in[1]; p.wo = (const char*)d_in[2];
  p.wg = (const char*)d_in[4]; p.wu = (const char*)d_in[5]; p.wd = (const char*)d_in[6]; p.gw = (const char*)d_in[7];
  p.out = (char*)d_out; p.ws = (char*)d_ws;
  fused_kernel<<<256, 512, 0, stream>>>(p);
}
```

```cpp
#include <hip/hip_runtime.h>
#include <hip/hip_bf16.h>
#include <type_traits>

#define LAS __attribute__((address_space(3)))
#define DI __device__ __forceinline__
#define IC(n) std::integral_constant<int, (n)>{}
#define SB __builtin_amdgcn_sched_barrier(0)
typedef short s16x4 __attribute__((ext_vector_type(4)));
typedef short s16x8 __attribute__((ext_vector_type(8)));
typedef __bf16 bf16x8 __attribute__((ext_vector_type(8)));
typedef __bf16 bf16x2 __attribute__((ext_vector_type(2)));
typedef float f32x4 __attribute__((ext_vector_type(4)));
typedef float f32x2 __attribute__((ext_vector_type(2)));
typedef unsigned v4u __attribute__((ext_vector_type(4)));
typedef unsigned v2u __attribute__((ext_vector_type(2)));
typedef __amdgpu_buffer_rsrc_t rsrc_t;

constexpr int S_ = 2048, D_ = 448, F_ = 1792;
constexpr unsigned XS = 896, QS = 2688, GS = 3584;
constexpr unsigned A2_OFF = 0, A2_RT = 72 * 1024;
constexpr unsigned QK_OFF = A2_OFF + 128 * A2_RT, QK_RP = 224 * 256;
constexpr unsigned G_OFF = QK_OFF + 64 * QK_RP;
constexpr unsigned ACK_OFF = G_OFF + 8192;
constexpr unsigned REQ_OFF = ACK_OFF + 256 * 128;
constexpr unsigned WS_BYTES = REQ_OFF + 256 * 256;
constexpr unsigned OOB = 0x80000000u;
constexpr unsigned KX0 = 0x9E3779B9u, KX1 = 0x7F4A7C15u;
constexpr unsigned SPIN_CAP = 1u << 16;
constexpr int SC1 = 16;
constexpr float LOG2E = 1.4426950408889634f;

struct Params {
  const char *x, *wqkv, *wg, *wu, *wd, *wo, *gw;
  char *out, *ws;
};
template <class T> DI T sel(bool c, T a, T b) { return c ? a : b; }
template <int I, int N, class Fn> DI void sfor(Fn&& f) {
  if constexpr (I < N) { f(std::integral_constant<int, I>{}); sfor<I + 1, N>(f); }
}
DI unsigned opaque(unsigned v) { asm("" : "+v"(v)); return v; }
DI int sfresh(int v) { asm volatile("" : "+s"(v)); return v; }
constexpr int CODEPF = 25600;
DI unsigned fetch_code(int line, unsigned long long pc) {
  unsigned d;
  asm volatile("global_load_dword %0, %1, %2" : "=v"(d) : "v"(line * 128 < CODEPF ? line * 128 : 0), "s"(pc) : "memory");
  return d;
}
DI rsrc_t mkrs(const void* p, unsigned n) { return __builtin_amdgcn_make_buffer_rsrc((void*)p, (short)0, n, 0x00020000); }
DI void dma1(unsigned m0, unsigned voff, rsrc_t rs, unsigned soff) {
  asm volatile("s_mov_b32 m0, %0\n\ts_nop 0\n\tbuffer_load_dwordx4 %1, %2, %3 offen lds" ::"s"(m0), "v"(voff), "s"(rs), "s"(soff) : "memory");
}
DI void dma4(unsigned m0, const unsigned (&v)[4], rsrc_t rs, unsigned soff) {
  asm volatile("s_mov_b32 m0, %0\n\ts_nop 0\n\t"
               "buffer_load_dwordx4 %1, %5, %6 offen lds\n\t"
               "buffer_load_dwordx4 %2, %5, %6 offen offset:1024 lds\n\t"
               "buffer_load_dwordx4 %3, %5, %6 offen offset:2048 lds\n\t"
               "buffer_load_dwordx4 %4, %5, %6 offen offset:3072 lds"
               ::"s"(m0), "v"(v[0]), "v"(v[1]), "v"(v[2]), "v"(v[3]), "s"(rs), "s"(soff) : "memory");
}
DI void dma2(unsigned m0, unsigned v0, unsigned v1, rsrc_t rs, unsigned soff) {
  asm volatile("s_mov_b32 m0, %0\n\ts_nop 0\n\t"
               "buffer_load_dwordx4 %1, %3, %4 offen lds\n\t"
               "buffer_load_dwordx4 %2, %3, %4 offen offset:1024 lds"
               ::"s"(m0), "v"(v0), "v"(v1), "s"(rs), "s"(soff) : "memory");
}
template <int N> DI void sync() { asm volatile("s_waitcnt vmcnt(%0) lgkmcnt(0)\n\ts_barrier" ::"i"(N > 63 ? 63 : N) : "memory"); }
DI void sync_lds() { asm volatile("s_waitcnt lgkmcnt(0)\n\ts_barrier" ::: "memory"); }
DI void bar() { asm volatile("s_barrier" ::: "memory"); }
DI v4u ld128(unsigned voff, rsrc_t rs, unsigned soff) {
  v4u r; asm volatile("buffer_load_dwordx4 %0, %1, %2, %3 offen sc1" : "=v"(r) : "v"(voff), "s"(rs), "s"(soff) : "memory"); return r;
}
DI v2u ld64(unsigned voff, rsrc_t rs) {
  v2u r; asm volatile("buffer_load_dwordx2 %0, %1, %2, 0 offen sc1" : "=v"(r) : "v"(voff), "s"(rs) : "memory"); return r;
}
DI unsigned ld32(unsigned voff, rsrc_t rs, unsigned soff) {
  unsigned r; asm volatile("buffer_load_dword %0, %1, %2, %3 offen sc1" : "=v"(r) : "v"(voff), "s"(rs), "s"(soff) : "memory"); return r;
}
template <bool S1> DI void st128(v4u v, unsigned voff, rsrc_t rs, unsigned soff) {
  if constexpr (S1) asm volatile("buffer_store_dwordx4 %0, %1, %2, %3 offen sc1\n\ts_nop 1" ::"v"(v), "v"(voff), "s"(rs), "s"(soff) : "memory");
  else asm volatile("buffer_store_dwordx4 %0, %1, %2, %3 offen\n\ts_nop 1" ::"v"(v), "v"(voff), "s"(rs), "s"(soff) : "memory");
}
DI void st64(v2u v, unsigned voff, rsrc_t rs) { __builtin_amdgcn_raw_buffer_store_b64(v, rs, voff, 0, SC1); }
template <int N, class T> DI void wait_for(T& a) { asm volatile("s_waitcnt vmcnt(%1)" : "+v"(a) : "i"(N) : "memory"); }
template <class T> DI void landed(T& a) { asm volatile("" : "+v"(a)::"memory"); }

template <int OFF> DI s16x4 tr(const unsigned (&b)[3]) {
  return __builtin_amdgcn_ds_read_tr16_b64_v4i16((LAS s16x4*)(size_t)(b[OFF >> 16] + (OFF & 0xFFFF)));
}
template <int OFF, int STEP> DI s16x8 tr2(const unsigned (&b)[3]) {
  return __builtin_shufflevector(tr<OFF>(b), tr<OFF + STEP>(b), 0, 1, 2, 3, 4, 5, 6, 7);
}
template <int OFF> DI s16x8 rd128(const unsigned (&b)[3]) { return *(const LAS s16x8*)(size_t)(b[OFF >> 16] + (OFF & 0xFFFF)); }
DI void seg3(unsigned (&b)[3], unsigned v) { b[0] = opaque(v); b[1] = opaque(v + 65536u); b[2] = opaque(v + 131072u); }
DI f32x4 mfma16x32(s16x8 a, s16x8 b, f32x4 c) {
  return __builtin_amdgcn_mfma_f32_16x16x32_bf16(__builtin_bit_cast(bf16x8, a), __builtin_bit_cast(bf16x8, b), c, 0, 0, 0);
}
DI f32x4 mfma16x16(v2u a, v2u b, f32x4 c) {
  return __builtin_amdgcn_mfma_f32_16x16x16bf16_1k(__builtin_bit_cast(s16x4, a), __builtin_bit_cast(s16x4, b), c, 0, 0, 0);
}
DI unsigned pk(float a, float b) { f32x2 p = {a, b}; return __builtin_bit_cast(unsigned, __builtin_convertvector(p, bf16x2)); }
DI float sigm(float g) { return __builtin_amdgcn_rcpf(1.0f + __builtin_amdgcn_exp2f(g * -LOG2E)); }
DI void swap16(unsigned& a, unsigned& b) { auto t = __builtin_amdgcn_permlane16_swap(a, b, false, false); const unsigned x = t[0], y = t[1]; a = x; b = y; }
DI void swap32(unsigned& a, unsigned& b) { auto t = __builtin_amdgcn_permlane32_swap(a, b, false, false); const unsigned x = t[0], y = t[1]; a = x; b = y; }
DI float vmax(float a, float b) { float r; asm("v_max_f32 %0, %1, %2" : "=v"(r) : "v"(a), "v"(b)); return r; }
DI float vmax_n(float a, float b) { float r; asm("v_max_f32 %0, %1, %2\n\ts_nop 1" : "=v"(r) : "v"(a), "v"(b)); return r; }
DI void regroup(v4u& lo, v4u& hi) {
  unsigned b = lo[2], c = hi[0];
  asm("v_swap_b32 %0, %1" : "+v"(b), "+v"(c));
  lo[2] = b; hi[0] = c;
  asm("" : "+v"(lo), "+v"(hi));
  b = lo[3]; c = hi[1];
  asm("v_swap_b32 %0, %1" : "+v"(b), "+v"(c));
  lo[3] = b; hi[1] = c;
}
DI float vmax3(float a, float b, float c) { float r; asm("v_max3_f32 %0, %1, %2, %3" : "=v"(r) : "v"(a), "v"(b), "v"(c)); return r; }
template <bool MAX, class Fn> DI void quad_reduce(unsigned (&x)[4], Fn&& fn) {
  auto op = [](unsigned a, unsigned b) {
    return __float_as_uint(MAX ? vmax_n(__uint_as_float(a), __uint_as_float(b)) : __uint_as_float(a) + __uint_as_float(b));
  };
  swap16(x[0], x[1]); swap16(x[2], x[3]);
  unsigned ab = op(x[0], x[1]), cd = op(x[2], x[3]);
  swap32(ab, cd);
  ab = __float_as_uint(fn(__uint_as_float(op(ab, cd)))); cd = ab;
  swap32(ab, cd);
  x[0] = x[1] = ab; x[2] = x[3] = cd;
  swap16(x[0], x[1]); swap16(x[2], x[3]);
}
DI int mod7(int v) { return v >= 7 ? v - 7 : v; }

template <int N1, int N2>
DI void gate_bar(unsigned p1, unsigned p2, unsigned lo, unsigned voff, rsrc_t rs) {
  unsigned p3, t, cnt;
  asm volatile(
      "s_waitcnt vmcnt(%9)\n\tv_sub_u32 %3, %0, %5\n\tv_cmp_lt_u32 vcc, 1, %3\n\ts_mov_b32 %4, %8\n\ts_cbranch_vccz 3f\n\t"
      "buffer_load_dword %0, %6, %7, 0 offen sc1\n\t"
      "s_waitcnt vmcnt(%10)\n\tv_sub_u32 %3, %1, %5\n\tv_cmp_lt_u32 vcc, 1, %3\n\ts_nop 0\n\ts_cbranch_vccz 2f\n\t"
      "buffer_load_dword %1, %6, %7, 0 offen sc1\n\ts_sleep 1\n\t"
      "buffer_load_dword %2, %6, %7, 0 offen sc1\n"
      "1:\n\t"
      "s_waitcnt vmcnt(2)\n\tv_sub_u32 %3, %0, %5\n\tv_cmp_lt_u32 vcc, 1, %3\n\ts_nop 0\n\ts_cbranch_vccz 2f\n\t"
      "buffer_load_dword %0, %6, %7, 0 offen sc1\n\t"
      "s_waitcnt vmcnt(2)\n\tv_sub_u32 %3, %1, %5\n\tv_cmp_lt_u32 vcc, 1, %3\n\ts_nop 0\n\ts_cbranch_vccz 2f\n\t"
      "buffer_load_dword %1, %6, %7, 0 offen sc1\n\t"
      "s_waitcnt vmcnt(2)\n\tv_sub_u32 %3, %2, %5\n\tv_cmp_lt_u32 vcc, 1, %3\n\ts_nop 0\n\ts_cbranch_vccz 2f\n\t"
      "buffer_load_dword %2, %6, %7, 0 offen sc1\n\t"
      "s_sub_u32 %4, %4, 1\n\ts_cbranch_scc0 1b\n"
      "2:\n\t"
      "s_barrier\n\ts_waitcnt vmcnt(0)\n\ts_branch 4f\n"
      "3:\n\t"
      "s_barrier\n\ts_waitcnt vmcnt(%11)\n"
      "4:"
      : "+v"(p1), "+v"(p2), "=&v"(p3), "=&v"(t), "=&s"(cnt)
      : "v"(lo), "v"(voff), "s"(rs), "i"(SPIN_CAP), "n"(N1 + 1), "n"(N2 + 1), "n"(N2) : "vcc", "scc", "memory");
}
DI void settle2(v2u& a, v2u ref, unsigned va, rsrc_t rs) {
  unsigned cnt;
  asm volatile("s_mov_b32 %1, %5\n1:\n\tv_cmp_ne_u64 vcc, %0, %2\n\ts_sub_u32 %1, %1, 1\n\ts_cbranch_vccz 9f\n\ts_cbranch_scc1 9f\n\t"
               "buffer_load_dwordx2 %0, %3, %4, 0 offen sc1\n\ts_waitcnt vmcnt(0)\n\ts_branch 1b\n9:"
               : "+v"(a), "=&s"(cnt) : "v"(ref), "v"(va), "s"(rs), "i"(SPIN_CAP) : "vcc", "scc", "memory");
}

constexpr int CPRIO = 2;
constexpr int ROTN = 3;

enum { KX, KG, KQ, KH, KU };
struct Op { int kind, idx, batch, slot; };
constexpr int XREG = 1;
constexpr Op OPS[] = {
    {KX, 0, -1, 10}, {KG, 0, -1, 12}, {KQ, 0, -1, 14}, {KX, 1, -1, 0}, {KG, 1, -1, 16},
    {KQ, 1, 0, 15}, {KX, 2, 0, 2}, {KG, 2, 0, 18}, {KQ, 2, 0, 9},
    {KX, 3, 1, 4}, {KG, 3, 1, 12}, {KQ, 3, 1, 14},
    {KX, 4, 2, 6}, {KG, 4, 2, 16}, {KQ, 4, 2, 15},
    {KX, 5, 3, 8}, {KG, 5, 3, 18},
    {KQ, 5, 4, 14}, {KX, 6, 4, 10}, {KG, 6, 4, 12},
    {KQ, 6, 5, 15}, {KH, 0, 5, -1}, {KH, 1, 6, -1}, {KH, 2, 7, -1}, {KH, 3, 7, -1}, {KH, 4, 8, -1},
    {KH, 5, 9, -1}, {KU, 0, 9, -1}, {KH, 6, 10, -1}, {KU, 1, 10, -1}, {KU, 2, 11, -1}, {KU, 3, 12, -1}, {KU, 4, 12, -1},
    {KU, 5, 13, -1}, {KU, 6, 13, -1}, {KU, 7, 16, -1}, {KU, 8, 16, -1}, {KU, 9, 16, -1},
    {KU, 10, 18, -1}, {KU, 11, 18, -1}, {KU, 12, 18, -1}, {KU, 13, 18, -1},
    {KU, 14, 20, -1}, {KU, 15, 20, -1}, {KU, 16, 22, -1}, {KU, 17, 22, -1}};
constexpr int SYS[] = {0, 1, 2, 3, 4, 5, 6, 7, 8, 9, 10, 11, 12, 13, 14, 16, 18, 20, 22, 24, 26, 28, 30};
constexpr int NOPS = sizeof(OPS) / sizeof(Op), NSY = sizeof(SYS) / sizeof(int);
constexpr bool is_sync(int k) { for (int i = 0; i < NSY; ++i) if (SYS[i] == k) return true; return false; }
constexpr int sync_of(int st) { while (!is_sync(st)) --st; return st; }
constexpr int sync_after(int st) { ++st; while (st < 40 && !is_sync(st)) ++st; return st; }
constexpr int sync_before(int k) { --k; while (k >= 0 && !is_sync(k)) --k; return k; }
constexpr int op_units(int o) { return OPS[o].kind == KQ ? 1 : 2; }
constexpr int op_vm(int o) { return 2 * op_units(o); }
constexpr int op_need(int o) {
  return sync_of(OPS[o].kind == KH ? 7 + OPS[o].idx : OPS[o].kind == KU ? 14 + OPS[o].idx : OPS[o].idx);
}
constexpr int op_free(int o) {
  return sync_after(OPS[o].kind == KX && OPS[o].idx >= XREG ? 7 + OPS[o].idx : op_need(o));
}
struct Alloc { int off[NOPS]; bool ok; };
constexpr Alloc make_alloc() {
  Alloc a{}; a.ok = true;
  int fr[20] = {};
  for (int i = 0; i < 20; ++i) fr[i] = -9;
  for (int o = 0; o < NOPS; ++o) {
    if (OPS[o].batch >= op_need(o)) a.ok = false;
    if (o > 0 && OPS[o].batch < OPS[o - 1].batch) a.ok = false;
    if (OPS[o].batch >= 0 && !is_sync(OPS[o].batch)) a.ok = false;
    int s = OPS[o].slot;
    if (s < 0) for (int i = 0; i < 20; i += 2) if (fr[i] <= OPS[o].batch && fr[i + 1] <= OPS[o].batch) { s = i; break; }
    if (s < 0) { a.ok = false; s = 0; }
    for (int i = 0; i < op_units(o); ++i) { if (fr[s + i] > OPS[o].batch) a.ok = false; fr[s + i] = op_free(o); }
    a.off[o] = s * 8192;
  }
  return a;
}
constexpr Alloc ALLOC = make_alloc();
static_assert(ALLOC.ok, "LDS schedule");
constexpr int find_op(int kind, int idx) { for (int o = 0; o < NOPS; ++o) if (OPS[o].kind == kind && OPS[o].idx == idx) return o; return -1; }
constexpr int lds_of(int kind, int idx) { return ALLOC.off[find_op(kind, idx)]; }
constexpr bool last_uses(int o) { for (int n = SYS[NSY - 1] - 14; n < 18; ++n) if (lds_of(KU, n) == o) return true; return false; }
constexpr int red_run(int from) {
  int o = from;
  while (last_uses(o) || last_uses(o + 16384)) o += 16384;
  return o;
}
static_assert(red_run(red_run(0) + 32768) + 32768 <= 163840, "");
constexpr int first_op(int batch) { int o = 0; while (o < NOPS && OPS[o].batch < batch) ++o; return o; }
constexpr int vm_between(int from, int to) { int n = 0; for (int o = from; o < to; ++o) n += op_vm(o); return n; }
constexpr int loader_wait(int k) {
  int last = -1;
  for (int o = 0; o < NOPS; ++o) if (op_need(o) <= k) last = o;
  const int n = vm_between(last + 1, first_op(k)), room = 60 - vm_between(first_op(k), first_op(k + 1));
  return n < room ? n : room;
}
constexpr int KPQ = 9, KP1 = 10, KP2 = 16, KG1 = 14, KG2 = 22, KG3 = 26;
static_assert(is_sync(KG1) && is_sync(KG2) && is_sync(KG3) && is_sync(KP1) && is_sync(KP2) && is_sync(KPQ), "");
static_assert(sync_before(sync_before(KG2)) >= KG1 && sync_before(sync_before(KG3)) >= KG2, "one gate at a time");
constexpr int lout(int P) {
  const int cap = P < KG2 - 15 ? 7 : P < KG3 - 15 ? 14 : 18, n = P < KG2 - 15 ? 7 : 10 + P - (KG2 - 15);
  return n < cap ? n : cap;
}

__global__ __launch_bounds__(768, 3) void fused_kernel(Params p) {
  __shared__ __attribute__((aligned(16))) char smem[163840];
  const unsigned lds0 = (unsigned)(size_t)(LAS char*)smem;
  const int tid = threadIdx.x, lane = tid & 63, w = __builtin_amdgcn_readfirstlane(tid >> 6);
  const int b = blockIdx.x, xcd = b & 7, bi = b >> 3;
  unsigned long long pc;
  asm volatile("s_getpc_b64 %0" : "=s"(pc));
  const bool isF = bi < 28;
  const int lin = bi + (xcd >> 1) * (isF ? 28 : 4) + (isF ? 0 : 84);
  const int s = lin >> 3, r = (xcd & 1) * 8 + (lin & 7), a = s - 14;
  const int c0 = (((r & 7) + 2 * s) % ROTN) * (7 / ROTN);
  constexpr int ASLOT = 51200, AGT = 3 * ASLOT;
#define TILE_VALUES                                                                                                  \
  const int bi_ = sfresh(bi), t2 = bi_ >= 14 ? bi_ - 14 : bi_, r2 = 2 * xcd + (bi_ >= 14 ? 1 : 0);                   \
  const int n2 = t2 >= 7 ? t2 - 7 : t2, rb2 = 2 * r2 + (t2 >= 7 ? 1 : 0);                                            \
  const int rot = (2 * n2 + (7 * (rb2 & 3)) / 2) % 14, rot8 = n2 + 2 * (rb2 & 3);                                    \
  auto p2unit = [&](int P) -> int {              \
    if (P < 28) { int v = (P % 14) + rot; v = v >= 14 ? v - 14 : v; return 2 * v + P / 14; }                         \
    return 28 + ((P - 28 + rot8) & 7);                                                                               \
  }

  if (__builtin_expect(w < 4, 1)) {
    const int l8 = lane >> 3, c8 = lane & 7, e = w >> 1, hw = w & 1;
    const rsrc_t rx = mkrs(p.x, S_ * D_ * 2);
    unsigned vx[4], vq[4];
    const unsigned m0w = lds0 + w * 4096;
    for (int i = 0; i < 4; ++i) vx[i] = (8 * i + l8) * XS + ((c8 ^ (4 * (i & 1) + (l8 >> 1))) << 4) - 1024 * i;
    dma4(m0w + (isF ? ALLOC.off[0] : 0), vx, rx, (r * 128 + 32 * w) * XS + c0 * 128);
    SB;
    const rsrc_t rq = mkrs(p.wqkv, D_ * 3 * D_ * 2), rw = mkrs(p.ws, WS_BYTES);
    for (int i = 0; i < 4; ++i) vq[i] = (8 * i + l8) * QS + ((c8 ^ (l8 & 2) ^ ((i & 1) << 2)) << 4) - 1024 * i;
    const bool pw = w == 0;
    const unsigned reqp0 = REQ_OFF + (r * 16 + s) * 256 + lane * 8;
    if (__builtin_expect(isF, 1)) {
      unsigned vg[4], vd[4];
      for (int i = 0; i < 4; ++i) vg[i] = vq[i] + (8 * i + l8) * (GS - QS);
      const rsrc_t rg = mkrs(e ? p.wu : p.wg, D_ * F_ * 2);
      static_assert(OPS[1].kind == KG && OPS[2].kind == KQ, "");
      dma4(m0w + ALLOC.off[1], vg, rg, (c0 * 64 + 32 * hw) * GS + s * 256);
      dma2(lds0 + ALLOC.off[2] + w * 2048, vq[0], vq[1], rq, (c0 * 64 + 16 * w) * QS + s * 128);
      SB;
      TILE_VALUES;
      for (int i = 0; i < 4; ++i) vd[i] = vq[i] - (8 * i + l8) * (QS - XS);
      const rsrc_t rd = mkrs(p.wd, F_ * D_ * 2), ro = mkrs(p.wo, D_ * D_ * 2);
      auto issue = [&](auto oc) {
        constexpr int O = decltype(oc)::value, K = OPS[O].kind, I = OPS[O].idx, off = ALLOC.off[O];
        if constexpr (K == KU) {
          const int v = e ? p2unit(2 * I + 1) : p2unit(2 * I);
          dma4(m0w + off, vd, I < 14 ? rd : ro, (I < 14 ? 64 * v + 32 * hw : 28 * (2 * (v - 28) + hw)) * XS + n2 * 128);
        } else {
          const int c = mod7(I + c0);
          if constexpr (K == KX) dma4(m0w + off, vx, rx, (r * 128 + 32 * w) * XS + c * 128);
          else if constexpr (K == KQ) dma2(lds0 + off + w * 2048, vq[0], vq[1], rq, (c * 64 + 16 * w) * QS + s * 128);
          else dma4(m0w + off, vg, rg, (c * 64 + 32 * hw) * GS + s * 256 + (K == KH ? 128 : 0));
        }
      };
      const int u = s >= 7 ? s - 7 : s, isk = s >= 7, aa = lane & 1, nxt = lane >= 16;
      const bool inr = u >= 3 * aa && u <= 3 * aa + 3;
      const bool pvalid = pw && (lane < 14 || (lane < 16 && inr) || (lane < 18 && inr && isk && r < 15));
      const unsigned ackc = sel(pw && lane < 16, ACK_OFF + (r2 * 14 + t2) * 128 + lane * 8, OOB);
      const unsigned reqc = sel(pw && lane < 16, REQ_OFF + (r2 * 16 + lane) * 256 + t2 * 8, OOB);
      const unsigned ackp = sel(pvalid, sel(lane < 14, ACK_OFF + (r * 14 + lane) * 128 + s * 8,
                                            ACK_OFF + (224 + 2 * (r + nxt) + aa) * 128 + (sel(nxt, 8, isk ? 4 : 0) + u - 3 * aa) * 8), OOB);
      const unsigned reqp = sel(pvalid, reqp0, OOB);
      v2u a0 = ld64(ackc, rw), pak = ld64(ackp, rw), poll;
      sfor<3, first_op(0)>(issue);
      unsigned p1, p2;
      const bool ish = pw && lane < 14, iso = pw && (lane == 14 || lane == 15);
      const unsigned vh_ = sel(ish, ackc, OOB), vo_ = sel(iso, ackc, OOB);
      sfor<0, NSY>([&](auto ic) {
        constexpr int K = SYS[decltype(ic)::value], K1 = sync_before(K), K2 = sync_before(K1), N1 = sync_after(K), N2 = sync_after(N1);
        if constexpr (K == 6) {
          wait_for<vm_between(first_op(4), first_op(6))>(poll);
          settle2(poll, v2u{sel(pvalid, pak.x ^ KX0, 0u), sel(pvalid, pak.y ^ KX1, 0u)}, reqp, rw);
        }
        if constexpr (K == KG1 || K == KG2 || K == KG3) {
          asm volatile("s_waitcnt vmcnt(%0)" ::"i"(loader_wait(K)) : "memory");
          gate_bar<vm_between(first_op(K2), first_op(K)), vm_between(first_op(K1), first_op(K))>(
              p1, p2, sel(K == KG3 ? iso : ish, a0.x + (K == KG2 ? 2u : 1u), 0u), K == KG3 ? vo_ : vh_, rw);
        } else sync<loader_wait(K) + (K == 0 ? 2 : 0)>();
        if constexpr (K == 1) { landed(a0); landed(pak); st64(v2u{a0.x ^ KX0, a0.y ^ KX1}, reqc, rw); }
        if constexpr (K == KPQ) st64(v2u{pak.x + 1u, pak.y}, sel(lane >= 14, ackp, OOB), rw);
        if constexpr (K == KP1) st64(v2u{pak.x + 1u, pak.y}, sel(lane < 14, ackp, OOB), rw);
        if constexpr (K == KP2) st64(v2u{pak.x + 2u, pak.y}, sel(lane < 14, ackp, OOB), rw);
        if constexpr (K == 4) poll = ld64(reqp, rw);
        if constexpr (N2 == KG1 || N2 == KG2 || N2 == KG3) p1 = ld32(N2 == KG3 ? vo_ : vh_, rw, 0);
        if constexpr (N1 == KG1 || N1 == KG2 || N1 == KG3) p2 = ld32(N1 == KG3 ? vo_ : vh_, rw, 0);
        sfor<first_op(K), first_op(K + 1)>(issue);
      });
    } else {
      const unsigned vh = (8 * hw + l8) * XS + ((c8 ^ (4 * hw + (l8 >> 1))) << 4);
      auto issue = [&](auto ic) {
        constexpr int I = decltype(ic)::value;
        const int c = mod7(I + c0);
        const unsigned m0 = m0w + (I % 3) * ASLOT;
        if constexpr (I > 0) dma4(m0, vx, rx, (r * 128 + 32 * w) * XS + c * 128);
        dma1(lds0 + (I % 3) * ASLOT + 49152 + hw * 1024, vh, rx, r > 0 ? (r * 128 - 16) * XS + c * 128 : OOB);
        dma4(m0 + 16384, vq, rq, (c * 64 + 32 * hw) * QS + 1792 + (3 * a + e) * 128);
        dma4(m0 + 32768, vq, rq, (c * 64 + 32 * hw) * QS + 1792 + (3 * a + 2 + e) * 128);
      };
      dma1(lds0 + AGT, lane * 16, mkrs(p.gw, D_ * 2), 0);
      issue(IC(0));
      const bool cvalid = pw && (lane < 8 || (lane < 12 && r > 0)), pvalid = pw && lane < 14;
      const int ps = sel(lane < 4, 3 * a + lane, 7 + 3 * a + (lane & 3)), pr = sel(lane < 8, r, r - 1);
      const unsigned ackc = sel(cvalid, ACK_OFF + (224 + 2 * r + a) * 128 + lane * 8, OOB);
      const unsigned reqc = sel(cvalid, REQ_OFF + (pr * 16 + ps) * 256 + (sel(lane < 8, 14, 16) + a) * 8, OOB);
      const unsigned ackp = sel(pvalid, ACK_OFF + (r * 14 + lane) * 128 + s * 8, OOB), reqp = sel(pvalid, reqp0, OOB);
      v2u a0 = ld64(ackc, rw), pak = ld64(ackp, rw), poll;
      issue(IC(1));
      sfor<0, 7>([&](auto kc) {
        constexpr int K = decltype(kc)::value;
        sync<(K < 6 ? 13 : 0)>();
        if constexpr (K == 0) { landed(a0); landed(pak); st64(v2u{a0.x ^ KX0, a0.y ^ KX1}, reqc, rw); }
        if constexpr (K == 4) poll = ld64(reqp, rw);
        if constexpr (K + 2 < 7) issue(IC(K + 2));
      });
      landed(poll);
      settle2(poll, v2u{sel(pvalid, pak.x ^ KX0, 0u), sel(pvalid, pak.y ^ KX1, 0u)}, reqp, rw);
      const unsigned p1 = ld32(ackc, rw, 0);
      __builtin_amdgcn_s_sleep(3);
      const unsigned p2 = ld32(ackc, rw, 0);
      __builtin_amdgcn_s_sleep(3);
      gate_bar<0, 0>(p1, p2, sel(cvalid, a0.x + 1u, 0u), ackc, rw);
      bar();
      st64(v2u{pak.x + 1u, pak.y}, ackp, rw);
    }
  } else if (isF) {
    __builtin_amdgcn_s_setprio(CPRIO);
    TILE_VALUES;
    unsigned pfd = fetch_code(bi * 8 + w - 4, pc);
    const int cw = w - 4, grp = lane >> 4, m16 = lane & 15, q4 = m16 >> 2, p4 = lane & 3;
    const rsrc_t rw = mkrs(p.ws, WS_BYTES);
    const int rp = cw >> 2, cb = cw & 3;
    const unsigned va2 = lane * 16;
    const unsigned f1 = (q4 >> 1) | ((grp & 1) << 1), rowp = lds0 + (8 * grp + q4) * 128 + p4 * 8;
    unsigned bx[2][3], bw[3];
    {
      const unsigned xr = lds0 + rp * 8192 + m16 * 128;
      seg3(bx[0], xr + ((grp ^ (m16 >> 1)) << 4));
      seg3(bx[1], xr + (((4 + grp) ^ (m16 >> 1)) << 4));
      seg3(bw, rowp + ((cb ^ f1) << 5));
    }
    s16x8 xk[XREG * 2][4], xf[2][4], wf[2][3];
    f32x4 a1[3][4], a2[2][4];
    sfor<0, 4>([&](auto i) { a1[0][i] = a1[1][i] = a1[2][i] = a2[0][i] = a2[1][i] = f32x4{0, 0, 0, 0}; });
    v4u al[18][2];
    auto rd1 = [&](auto tc, auto jc) {
      constexpr int T = decltype(tc)::value, J = decltype(jc)::value, st = (T % 14) / 2, kk = T & 1;
      if constexpr (T < 28) {
        if constexpr (J < 2) wf[kk][J] = tr2<lds_of(T < 14 ? KG : KH, st) + J * 8192 + kk * 4096, 512>(bw);
        else if constexpr (J == 2) { if constexpr (T < 14) wf[kk][2] = tr2<lds_of(KQ, st) + kk * 4096, 512>(bw); }
        else if constexpr (st < XREG) { if constexpr (T < 14) xk[T][J - 3] = rd128<lds_of(KX, st) + (J - 3) * 2048>(bx[kk]); }
        else xf[kk][J - 3] = rd128<lds_of(KX, st) + (J - 3) * 2048>(bx[kk]);
      }
    };
    auto xop = [&](auto tc, auto ic) -> s16x8 {
      constexpr int T = decltype(tc)::value, I = decltype(ic)::value;
      if constexpr ((T % 14) / 2 < XREG) return xk[T % 14][I]; else return xf[T & 1][I];
    };
    auto issue_load = [&](auto lc) {
      constexpr int L = decltype(lc)::value;
      const int v = (cb >> 1) ? p2unit(2 * L + 1) : p2unit(2 * L);
      const unsigned so = A2_OFF + (rb2 * 4 + rp * 2) * A2_RT + (2 * v + (cb & 1)) * 1024;
      al[L][0] = ld128(va2, rw, so);
      al[L][1] = ld128(va2, rw, so + A2_RT);
    };
    constexpr int ORD[7] = {0, 3, 1, 2, 4, 5, 6};
    auto step1 = [&](auto tc) {
      constexpr int T = decltype(tc)::value, kk = T & 1;
      sfor<0, 4>([&](auto i) {
        constexpr int I = decltype(i)::value;
        if constexpr (T < 14) {
          a1[0][i] = mfma16x32(wf[kk][0], xop(tc, i), a1[0][i]); SB;
          rd1(IC(T + 1), IC(ORD[2 * I])); SB;
          a1[1][i] = mfma16x32(wf[kk][1], xop(tc, i), a1[1][i]); SB;
          if constexpr (2 * I + 1 < 7) { rd1(IC(T + 1), IC(ORD[(2 * I + 1) % 7])); SB; }
          a1[2][i] = mfma16x32(wf[kk][2], xop(tc, i), a1[2][i]); SB;
        } else {
          a2[0][i] = mfma16x32(wf[kk][0], xop(tc, i), a2[0][i]); SB;
          rd1(IC(T + 1), IC(ORD[2 * I])); SB;
          a2[1][i] = mfma16x32(wf[kk][1], xop(tc, i), a2[1][i]); SB;
          if constexpr (2 * I + 1 < 7) { rd1(IC(T + 1), IC(ORD[(2 * I + 1) % 7])); SB; }
        }
      });
    };
    const unsigned vh = (grp & 1) * A2_RT + (grp >> 1) * 256 + m16 * 16;
    auto store_h = [&](const f32x4 (&g)[4], const f32x4 (&u)[4], int pair) {
      sfor<0, 2>([&](auto e) {
        float h[8];
        sfor<0, 8>([&](auto n) { h[n] = sigm(g[2 * e + n / 4][n % 4]) * u[2 * e + n / 4][n % 4]; });
        unsigned a0 = pk(h[0], h[1]), a1_ = pk(h[2], h[3]), b0 = pk(h[4], h[5]), b1 = pk(h[6], h[7]);
        swap16(a0, b0); swap16(a1_, b1);
        st128<true>(v4u{a0, a1_, b0, b1}, vh, rw, A2_OFF + (r * 8 + 4 * rp + 2 * e) * A2_RT + (4 * s + 2 * pair) * 1024 + cb * 512);
      });
    };
    bar();
    sfor<0, 7>([&](auto j) { rd1(IC(0), IC(ORD[j])); });
    SB;
    sfor<0, 14>([&](auto ic) {
      constexpr int I = decltype(ic)::value, K = I + 1;
      if constexpr (I == 2) wait_for<0>(pfd);
      if constexpr (I == 7) {
        sfor<0, 2>([&](auto e) {
          const f32x4 &c0_ = a1[2][2 * e], &c1 = a1[2][2 * e + 1];
          st128<true>(v4u{pk(c0_[0], c0_[1]), pk(c0_[2], c0_[3]), pk(c1[0], c1[1]), pk(c1[2], c1[3])}, va2, rw,
                      QK_OFF + (r * 4 + 2 * rp + e) * QK_RP + (16 * s + 4 * cb) * 256);
        });
        store_h(a1[0], a1[1], 0);
        SB;
      }
      step1(IC(2 * I));
      if constexpr (K < 14 && is_sync(K)) {
        sync<(K == KP1 ? 0 : K == KPQ ? 2 : 63)>();
        SB;
      }
      step1(IC(2 * I + 1));
    });
    store_h(a2[0], a2[1], 1);

    unsigned b2[4][3];
    sfor<0, 4>([&](auto t) { seg3(b2[t], rowp + cb * 4096 + ((((t + cb) & 3) ^ f1) << 5)); });
    f32x4 oh[2][4];
    sfor<0, 4>([&](auto t) { oh[0][t] = oh[1][t] = f32x4{0, 0, 0, 0}; });
    constexpr int RED = red_run(0), RED2 = red_run(RED + 32768);
    LAS char* rbase = (LAS char*)smem + (rp ? RED2 : RED) + lane * 16;
    auto send = [&](auto t) {
      LAS char* d = rbase + ((t + cb) & 3) * 8192 + t * 2048;
      *(LAS f32x4*)d = oh[0][t];
      *(LAS f32x4*)(d + 1024) = oh[1][t];
    };
    s16x8 wv[2][4];
    unsigned gb0, gb1;
    auto rd2 = [&](auto pc, auto t) {
      constexpr int P = decltype(pc)::value;
      if constexpr (P < 18) wv[P & 1][t] = tr2<lds_of(KU, P), 512>(b2[t]);
    };
    static_assert(KG1 == 14 && KP2 > 14, "");
    sync_lds();
    sfor<0, 7>(issue_load);
    sfor<0, 4>([&](auto t) { rd2(IC(0), t); });
    SB;
    sfor<0, 18>([&](auto pc) {
      constexpr int P = decltype(pc)::value, K = 15 + P;
      if constexpr (is_sync(K)) {
        sync<(K == KP2 ? 2 * lout(P - 1) : 63)>();
        if constexpr (K == KG3) {
          gb0 = ld32(m16 * 4, rw, G_OFF + (rb2 * 64 + rp * 32) * 4);
          gb1 = ld32(m16 * 4, rw, G_OFF + (rb2 * 64 + rp * 32 + 16) * 4);
        }
      }
      sfor<lout(P - 1), lout(P)>(issue_load);
      SB;
      static_assert(lout(P) > P, "load order");
      wait_for<2 * (lout(P) - 1 - P)>(al[P][1]);
      landed(al[P][0]);
      const s16x8 av0 = __builtin_bit_cast(s16x8, al[P][0]), av1 = __builtin_bit_cast(s16x8, al[P][1]);
      if constexpr (P == 14) {
        landed(gb0); landed(gb1);
        const float g0 = __uint_as_float(gb0), g1 = __uint_as_float(gb1);
        sfor<0, 4>([&](auto t) { oh[0][t] *= g0; oh[1][t] *= g1; });
        SB;
      }
      sfor<0, 4>([&](auto tt) {
        constexpr int t = P == 17 ? (tt + 1) & 3 : tt;
        oh[0][t] = mfma16x32(wv[P & 1][t], av0, oh[0][t]); oh[1][t] = mfma16x32(wv[P & 1][t], av1, oh[1][t]);
        SB;
        rd2(IC(P + 1), IC(P == 16 ? (tt + 1) & 3 : tt));
        if constexpr (P == 17 && tt > 0) send(tt);
        SB;
      });
    });
    sync_lds();
    f32x4 e0 = oh[0][0], e1 = oh[1][0];
    sfor<1, 4>([&](auto t) {
      e0 += *(const LAS f32x4*)(rbase + cb * 8192 + t * 2048);
      e1 += *(const LAS f32x4*)(rbase + cb * 8192 + t * 2048 + 1024);
    });
    unsigned a0 = pk(e0[0], e0[1]), a1_ = pk(e0[2], e0[3]), b0 = pk(e1[0], e1[1]), b1 = pk(e1[2], e1[3]);
    swap16(a0, b0); swap16(a1_, b1);
    st128<false>(v4u{a0, a1_, b0, b1}, ((grp & 1) * 16 + m16) * XS + (grp >> 1) * 16, mkrs(p.out, S_ * D_ * 2),
                 (rb2 * 64 + rp * 32) * XS + n2 * 128 + cb * 32);
  } else {
    unsigned pfd = fetch_code(bi * 8 + w - 4, pc);
    __builtin_amdgcn_s_setprio(CPRIO);
    const int cw = w - 4, grp = lane >> 4, m16 = lane & 15, q4 = m16 >> 2, p4 = lane & 3;
    const rsrc_t rw = mkrs(p.ws, WS_BYTES);
    unsigned bv0[3], bv1[3], bx0[3], bx1[3];
    {
      const unsigned f1 = (q4 >> 1) | ((grp & 1) << 1);
      const unsigned rowp = lds0 + 16384 + (8 * grp + q4) * 128;
      const int lc0 = 28 * cw + 32 * a + 4 * p4, lc1 = 28 * cw + 32 * a + 16 + (p4 == 3 ? 8 : 4 * p4);
      seg3(bv0, rowp + (lc0 >> 6) * 8192 + ((((lc0 >> 4) & 3) ^ f1) << 5) + (lc0 & 15) * 2);
      seg3(bv1, rowp + (lc1 >> 6) * 8192 + ((((lc1 >> 4) & 3) ^ f1) << 5) + (lc1 & 15) * 2);
      const unsigned xr0 = lds0 + m16 * 128;
      seg3(bx0, xr0 + ((grp ^ (m16 >> 1)) << 4));
      seg3(bx1, xr0 + (((4 + grp) ^ (m16 >> 1)) << 4));
    }
    constexpr int XR = 5;
    s16x8 xf[XR], vf[2][2], gf[2];
    f32x4 av[9][2], ag = {0, 0, 0, 0};
    sfor<0, 9>([&](auto j) { av[j][0] = f32x4{0, 0, 0, 0}; av[j][1] = f32x4{0, 0, 0, 0}; });
    auto rdx = [&](auto nc) {
      constexpr int N = decltype(nc)::value, T = N / 10, J = N % 10, kk = T & 1, so = ((T / 2) % 3) * ASLOT;
      if constexpr (T < 14) {
        if constexpr (J == 0) xf[N % XR] = kk ? rd128<so + 49152>(bx1) : rd128<so + 49152>(bx0);
        else if constexpr (J < 9) xf[N % XR] = kk ? rd128<so + (J - 1) * 2048>(bx1) : rd128<so + (J - 1) * 2048>(bx0);
        else xf[N % XR] = *(const LAS s16x8*)(size_t)((kk ? bx1[so >> 16] : bx0[so >> 16]) + cw * 2048 + (so & 0xFFFF));
      }
    };
    auto rdw = [&](auto tc) {
      constexpr int T = decltype(tc)::value, kk = T & 1, so = ((T / 2) % 3) * ASLOT;
      if constexpr (T < 14) {
        vf[kk][0] = tr2<so + kk * 4096, 512>(bv0);
        vf[kk][1] = tr2<so + kk * 4096, 512>(bv1);
        gf[kk] = *(const LAS s16x8*)(size_t)(lds0 + AGT + mod7(T / 2 + c0) * 128 + kk * 64 + grp * 16);
      }
    };
    bar();
    rdw(IC(0));
    sfor<0, XR - 1>(rdx);
    SB;
    sfor<0, 140>([&](auto nc) {
      constexpr int N = decltype(nc)::value, T = N / 10, J = N % 10, kk = T & 1;
      if constexpr ((N + XR - 1) % 20 == 0 && N + XR - 1 < 140) { sync_lds(); SB; }
      if constexpr (N == 30) wait_for<0>(pfd);
      if constexpr ((N + XR - 1) % 10 == 0) rdw(IC(T + 1));
      rdx(IC(N + XR - 1));
      SB;
      if constexpr (J < 9) {
        av[J][0] = mfma16x32(xf[N % XR], vf[kk][0], av[J][0]);
        av[J][1] = mfma16x32(xf[N % XR], vf[kk][1], av[J][1]);
      } else ag = mfma16x32(gf[kk], xf[N % XR], ag);
      SB;
    });
    v4u vp[5][2];
    sfor<0, 9>([&](auto j) {
      vp[j / 2][0][2 * (j & 1)] = pk(av[j][0][0], av[j][0][1]); vp[j / 2][0][2 * (j & 1) + 1] = pk(av[j][0][2], av[j][0][3]);
      vp[j / 2][1][2 * (j & 1)] = sel(m16 < 12, pk(av[j][1][0], av[j][1][1]), 0u);
      vp[j / 2][1][2 * (j & 1) + 1] = sel(m16 < 12, pk(av[j][1][2], av[j][1][3]), 0u);
    });
    vp[4][0][2] = vp[4][0][3] = vp[4][1][2] = vp[4][1][3] = 0u;
    const float gme = sigm(ag[0]);
    unsigned gsig = __float_as_uint(1.0f - gme);
    *(LAS float*)(size_t)(lds0 + AGT + 1024 + cw * 64 + m16 * 4) = gme;
    const int par = lane & 1;
    const bool curA = 4 * grp + par <= m16, curB = 4 * grp + 2 + par <= m16;
    constexpr float NEG = -3.0e38f, SCALE = 0.18898223650461361f;
    f32x4 bc = {sel(!par && curA, 0.f, NEG), sel(par && curA, 0.f, NEG), sel(!par && curB, 0.f, NEG), sel(par && curB, 0.f, NEG)};
    f32x4 bp = {sel(!par && !curA, 0.f, NEG), sel(par && !curA, 0.f, NEG), sel(!par && !curB, 0.f, NEG), sel(par && !curB, 0.f, NEG)};
    f32x4 bp0 = bp + sel(r == 0, -1e9f / SCALE, 0.f);
    const unsigned sa = par ? 0x01000c0cu : 0x0c0c0100u, sb = par ? 0x03020c0cu : 0x0c0c0302u;
    unsigned spa = sel(curA, 0x0c0c0c0cu, sa), sca = sel(curA, sa, 0x0c0c0c0cu), spb = sel(curB, 0x0c0c0c0cu, sb), scb = sel(curB, sb, 0x0c0c0c0cu);
    landed(gsig); landed(bc); landed(bp); landed(bp0); landed(spa); landed(sca); landed(spb); landed(scb);
    sfor<0, 5>([&](auto j) { landed(vp[j][0]); landed(vp[j][1]); });
    SB;
    sync_lds();
    const int hd = 8 * a + cw;
    const unsigned vlo = lane * 16, vhi = sel(grp < 3, 1024u + lane * 16, OOB);
    v4u ql[5], qh[5], kl[5], kh[5];
    auto ldqk = [&](auto pc) {
      constexpr int P = decltype(pc)::value;
      const unsigned base = QK_OFF + (r * 4 + P - 1) * QK_RP + 7 * hd * 256;
      const unsigned kb = (P == 0 && r == 0) ? OOB : base + 112 * 256;
      kl[P] = ld128(vlo, rw, kb);
      kh[P] = ld128((P == 0 && r == 0) ? OOB : vhi, rw, (P == 0 && r == 0) ? 0u : kb);
      if constexpr (P > 0) { ql[P] = ld128(vlo, rw, base); qh[P] = ld128(vhi, rw, base); }
    };
    sfor<0, 5>(ldqk);
    const float gq[2] = {*(const LAS float*)(size_t)(lds0 + AGT + 1024 + lane * 4), *(const LAS float*)(size_t)(lds0 + AGT + 1280 + lane * 4)};
    const unsigned vo = ((grp & 1) * 2 + (grp >> 1)) * 256 + m16 * 16;
    auto frag = [&](const v4u& lo, const v4u& hi, int e) { return __builtin_bit_cast(s16x8, e ? hi : lo); };
    SB;
    sfor<0, 2>([&](auto hc) {
      constexpr int H = decltype(hc)::value;
      wait_for<(H == 0 ? 8 : 4)>(qh[2 * H + 2]);
      sfor<2 * H + 1, 2 * H + 3>([&](auto P) { landed(kl[P]); landed(kh[P]); landed(ql[P]); landed(qh[P]); });
      if constexpr (H == 0) { landed(kl[0]); landed(kh[0]); regroup(kl[0], kh[0]); }
      sfor<2 * H + 1, 2 * H + 3>([&](auto P) { regroup(kl[P], kh[P]); regroup(ql[P], qh[P]); });
      SB;
      asm volatile("s_nop 1");
      SB;
      f32x4 sp[4], su[4];
      sfor<0, 4>([&](auto j) {
        constexpr int I = 4 * H + j, P = 1 + I / 2, e = I & 1, PP = e ? P : P - 1, ep = e ^ 1;
        const s16x8 q = frag(ql[P], qh[P], e);
        sp[j] = mfma16x32(frag(kl[PP], kh[PP], ep), q, I == 0 ? bp0 : bp);
        su[j] = mfma16x32(frag(kl[P], kh[P], e), q, bc);
      });
      SB;
      asm volatile("s_nop 7");
      SB;
      float la[4], lb[4]; unsigned x[4];
      sfor<0, 4>([&](auto j) { la[j] = vmax3(su[j][0], su[j][1], sp[j][0]); lb[j] = vmax3(su[j][2], su[j][3], sp[j][2]); });
      SB;
      sfor<0, 4>([&](auto j) { la[j] = vmax(la[j], sp[j][1]); lb[j] = vmax(lb[j], sp[j][3]); });
      SB;
      sfor<0, 4>([&](auto j) { x[j] = __float_as_uint(vmax_n(la[j], lb[j])); });
      quad_reduce<true>(x, [&](float m) { return m * -(SCALE * LOG2E); });
      sfor<0, 4>([&](auto j) {
        la[j] = __builtin_amdgcn_exp2f(__builtin_fmaf(la[j], SCALE * LOG2E, __uint_as_float(x[j])));
        lb[j] = __builtin_amdgcn_exp2f(__builtin_fmaf(lb[j], SCALE * LOG2E, __uint_as_float(x[j])));
      });
      SB;
      sfor<0, 4>([&](auto j) { x[j] = __float_as_uint(la[j] + lb[j]); });
      quad_reduce<false>(x, [&](float t) { return __builtin_amdgcn_rcpf(t) * gq[H]; });
      v4u pr_[4];
      sfor<0, 4>([&](auto j) {
        const unsigned t = pk(la[j] * __uint_as_float(x[j]), lb[j] * __uint_as_float(x[j]));
        pr_[j] = v4u{__builtin_amdgcn_perm(t, t, spa), __builtin_amdgcn_perm(t, t, spb), __builtin_amdgcn_perm(t, t, sca), __builtin_amdgcn_perm(t, t, scb)};
      });
      SB;
      f32x4 o_[4][2];
      sfor<0, 4>([&](auto j) {
        constexpr int I = 4 * H + j;
        sfor<0, 2>([&](auto t) {
          if constexpr ((I & 1) == 0) o_[j][t] = mfma16x32(__builtin_bit_cast(s16x8, vp[I / 2][t]), __builtin_bit_cast(s16x8, pr_[j]), f32x4{0, 0, 0, 0});
          else o_[j][t] = mfma16x16(v2u{vp[I / 2][t][2], vp[I / 2][t][3]}, v2u{pr_[j][0], pr_[j][1]}, f32x4{0, 0, 0, 0});
        });
      });
      SB;
      sfor<0, 4>([&](auto j) {
        constexpr int I = 4 * H + j;
        if constexpr (I & 1) sfor<0, 2>([&](auto t) { o_[j][t] = mfma16x16(v2u{vp[I / 2 + 1][t][0], vp[I / 2 + 1][t][1]}, v2u{pr_[j][2], pr_[j][3]}, o_[j][t]); });
      });
      SB;
      sfor<0, 4>([&](auto jj) {
        constexpr int j = (jj & 1) * 2 + jj / 2;
        unsigned a0 = pk(o_[j][0][0], o_[j][0][1]), a1 = pk(o_[j][0][2], o_[j][0][3]);
        unsigned b0 = pk(o_[j][1][0], o_[j][1][1]), b1 = pk(o_[j][1][2], o_[j][1][3]);
        swap16(a0, b0); swap16(a1, b1);
        st128<true>(v4u{a0, a1, b0, b1}, vo, rw, A2_OFF + (r * 8 + 4 * H + j) * A2_RT + (56 + hd) * 1024);
      });
      SB;
    });
    __builtin_amdgcn_raw_buffer_store_b32(gsig, rw, (a == 0 && lane < 16) ? lane * 4 : OOB, G_OFF + (r * 128 + cw * 16) * 4, SC1);
    sync<0>();
  }
}

extern "C" void kernel_launch(void* const* d_in, const int* in_sizes, int n_in, void* d_out, int out_size, void* d_ws, size_t ws_size,
                              hipStream_t stream) {
  Params p;
  p.x = (const char*)d_in[0]; p.wqkv = (const char*)d_in[1]; p.wo = (const char*)d_in[2];
  p.wg = (const char*)d_in[4]; p.wu = (const char*)d_in[5]; p.wd = (const char*)d_in[6]; p.gw = (const char*)d_in[7];
  p.out = (char*)d_out; p.ws = (char*)d_ws;
  fused_kernel<<<256, 768, 0, stream>>>(p);
}
```

```cpp
#include <hip/hip_runtime.h>
#include <hip/hip_bf16.h>
#include <type_traits>

#define LAS __attribute__((address_space(3)))
#define DI __device__ __forceinline__
#define IC(n) std::integral_constant<int, (n)>{}
#define SB __builtin_amdgcn_sched_barrier(0)
typedef short s16x4 __attribute__((ext_vector_type(4)));
typedef short s16x8 __attribute__((ext_vector_type(8)));
typedef __bf16 bf16x8 __attribute__((ext_vector_type(8)));
typedef __bf16 bf16x2 __attribute__((ext_vector_type(2)));
typedef float f32x4 __attribute__((ext_vector_type(4)));
typedef float f32x2 __attribute__((ext_vector_type(2)));
typedef unsigned v4u __attribute__((ext_vector_type(4)));
typedef unsigned v2u __attribute__((ext_vector_type(2)));
typedef __amdgpu_buffer_rsrc_t rsrc_t;

constexpr int S_ = 2048, D_ = 448, F_ = 1792;
constexpr unsigned XS = 896, QS = 2688, GS = 3584;
constexpr unsigned A2_OFF = 0, A2_RT = 72 * 1024;
constexpr unsigned QK_OFF = A2_OFF + 128 * A2_RT, QK_RP = 224 * 256;
constexpr unsigned G_OFF = QK_OFF + 64 * QK_RP;
constexpr unsigned ACK_OFF = G_OFF + 8192;
constexpr unsigned REQ_OFF = ACK_OFF + 256 * 128;
constexpr unsigned WS_BYTES = REQ_OFF + 256 * 256;
constexpr unsigned OOB = 0x80000000u;
constexpr unsigned KX0 = 0x9E3779B9u, KX1 = 0x7F4A7C15u;
constexpr unsigned SPIN_CAP = 1u << 16;
constexpr int SC1 = 16;
constexpr float LOG2E = 1.4426950408889634f;

struct Params {
  const char *x, *wqkv, *wg, *wu, *wd, *wo, *gw;
  char *out, *ws;
};
template <class T> DI T sel(bool c, T a, T b) { return c ? a : b; }
template <int I, int N, class Fn> DI void sfor(Fn&& f) {
  if constexpr (I < N) { f(std::integral_constant<int, I>{}); sfor<I + 1, N>(f); }
}
DI unsigned opaque(unsigned v) { asm("" : "+v"(v)); return v; }
DI int sfresh(int v) { asm volatile("" : "+s"(v)); return v; }
constexpr int CODEPF = 26368;
DI unsigned fetch_code(int line, unsigned long long pc) {
  unsigned d;
  asm volatile("global_load_dword %0, %1, %2" : "=v"(d) : "v"(line * 128 < CODEPF ? line * 128 : 0), "s"(pc) : "memory");
  return d;
}
DI rsrc_t mkrs(const void* p, unsigned n) { return __builtin_amdgcn_make_buffer_rsrc((void*)p, (short)0, n, 0x00020000); }
DI void dma1(unsigned m0, unsigned voff, rsrc_t rs, unsigned soff) {
  asm volatile("s_mov_b32 m0, %0\n\ts_nop 0\n\tbuffer_load_dwordx4 %1, %2, %3 offen lds" ::"s"(m0), "v"(voff), "s"(rs), "s"(soff) : "memory");
}
DI void dma4(unsigned m0, const unsigned (&v)[4], rsrc_t rs, unsigned soff) {
  asm volatile("s_mov_b32 m0, %0\n\ts_nop 0\n\t"
               "buffer_load_dwordx4 %1, %5, %6 offen lds\n\t"
               "buffer_load_dwordx4 %2, %5, %6 offen offset:1024 lds\n\t"
               "buffer_load_dwordx4 %3, %5, %6 offen offset:2048 lds\n\t"
               "buffer_load_dwordx4 %4, %5, %6 offen offset:3072 lds"
               ::"s"(m0), "v"(v[0]), "v"(v[1]), "v"(v[2]), "v"(v[3]), "s"(rs), "s"(soff) : "memory");
}
DI void dma2(unsigned m0, unsigned v0, unsigned v1, rsrc_t rs, unsigned soff) {
  asm volatile("s_mov_b32 m0, %0\n\ts_nop 0\n\t"
               "buffer_load_dwordx4 %1, %3, %4 offen lds\n\t"
               "buffer_load_dwordx4 %2, %3, %4 offen offset:1024 lds"
               ::"s"(m0), "v"(v0), "v"(v1), "s"(rs), "s"(soff) : "memory");
}
template <int N> DI void sync() { asm volatile("s_waitcnt vmcnt(%0) lgkmcnt(0)\n\ts_barrier" ::"i"(N > 63 ? 63 : N) : "memory"); }
DI void sync_lds() { asm volatile("s_waitcnt lgkmcnt(0)\n\ts_barrier" ::: "memory"); }
DI void bar() { asm volatile("s_barrier" ::: "memory"); }
DI v4u ld128(unsigned voff, rsrc_t rs, unsigned soff) {
  v4u r; asm volatile("buffer_load_dwordx4 %0, %1, %2, %3 offen sc1" : "=v"(r) : "v"(voff), "s"(rs), "s"(soff) : "memory"); return r;
}
DI v2u ld64(unsigned voff, rsrc_t rs) {
  v2u r; asm volatile("buffer_load_dwordx2 %0, %1, %2, 0 offen sc1" : "=v"(r) : "v"(voff), "s"(rs) : "memory"); return r;
}
DI unsigned ld32(unsigned voff, rsrc_t rs, unsigned soff) {
  unsigned r; asm volatile("buffer_load_dword %0, %1, %2, %3 offen sc1" : "=v"(r) : "v"(voff), "s"(rs), "s"(soff) : "memory"); return r;
}
template <bool S1> DI void st128(v4u v, unsigned voff, rsrc_t rs, unsigned soff) {
  if constexpr (S1) asm volatile("buffer_store_dwordx4 %0, %1, %2, %3 offen sc1\n\ts_nop 1" ::"v"(v), "v"(voff), "s"(rs), "s"(soff) : "memory");
  else asm volatile("buffer_store_dwordx4 %0, %1, %2, %3 offen\n\ts_nop 1" ::"v"(v), "v"(voff), "s"(rs), "s"(soff) : "memory");
}
DI void st64(v2u v, unsigned voff, rsrc_t rs) { __builtin_amdgcn_raw_buffer_store_b64(v, rs, voff, 0, SC1); }
template <int N, class T> DI void wait_for(T& a) { asm volatile("s_waitcnt vmcnt(%1)" : "+v"(a) : "i"(N) : "memory"); }
template <class T> DI void landed(T& a) { asm volatile("" : "+v"(a)::"memory"); }

template <int OFF> DI s16x4 tr(const unsigned (&b)[3]) {
  return __builtin_amdgcn_ds_read_tr16_b64_v4i16((LAS s16x4*)(size_t)(b[OFF >> 16] + (OFF & 0xFFFF)));
}
template <int OFF, int STEP> DI s16x8 tr2(const unsigned (&b)[3]) {
  return __builtin_shufflevector(tr<OFF>(b), tr<OFF + STEP>(b), 0, 1, 2, 3, 4, 5, 6, 7);
}
template <int OFF> DI s16x8 rd128(const unsigned (&b)[3]) { return *(const LAS s16x8*)(size_t)(b[OFF >> 16] + (OFF & 0xFFFF)); }
DI void seg3(unsigned (&b)[3], unsigned v) { b[0] = opaque(v); b[1] = opaque(v + 65536u); b[2] = opaque(v + 131072u); }
DI f32x4 mfma16x32(s16x8 a, s16x8 b, f32x4 c) {
  return __builtin_amdgcn_mfma_f32_16x16x32_bf16(__builtin_bit_cast(bf16x8, a), __builtin_bit_cast(bf16x8, b), c, 0, 0, 0);
}
DI f32x4 mfma16x16(v2u a, v2u b, f32x4 c) {
  return __builtin_amdgcn_mfma_f32_16x16x16bf16_1k(__builtin_bit_cast(s16x4, a), __builtin_bit_cast(s16x4, b), c, 0, 0, 0);
}
DI unsigned pk(float a, float b) { f32x2 p = {a, b}; return __builtin_bit_cast(unsigned, __builtin_convertvector(p, bf16x2)); }
DI float sigm(float g) { return __builtin_amdgcn_rcpf(1.0f + __builtin_amdgcn_exp2f(g * -LOG2E)); }
DI void swap16(unsigned& a, unsigned& b) { auto t = __builtin_amdgcn_permlane16_swap(a, b, false, false); const unsigned x = t[0], y = t[1]; a = x; b = y; }
DI void swap32(unsigned& a, unsigned& b) { auto t = __builtin_amdgcn_permlane32_swap(a, b, false, false); const unsigned x = t[0], y = t[1]; a = x; b = y; }
DI float vmax(float a, float b) { float r; asm("v_max_f32 %0, %1, %2" : "=v"(r) : "v"(a), "v"(b)); return r; }
DI float vmax_n(float a, float b) { float r; asm("v_max_f32 %0, %1, %2\n\ts_nop 1" : "=v"(r) : "v"(a), "v"(b)); return r; }
DI void regroup(v4u& lo, v4u& hi) {
  unsigned b = lo[2], c = hi[0];
  asm("v_swap_b32 %0, %1" : "+v"(b), "+v"(c));
  lo[2] = b; hi[0] = c;
  asm("" : "+v"(lo), "+v"(hi));
  b = lo[3]; c = hi[1];
  asm("v_swap_b32 %0, %1" : "+v"(b), "+v"(c));
  lo[3] = b; hi[1] = c;
}
DI float vmax3(float a, float b, float c) { float r; asm("v_max3_f32 %0, %1, %2, %3" : "=v"(r) : "v"(a), "v"(b), "v"(c)); return r; }
template <bool MAX, class Fn> DI void quad_reduce(unsigned (&x)[4], Fn&& fn) {
  auto op = [](unsigned a, unsigned b) {
    return __float_as_uint(MAX ? vmax_n(__uint_as_float(a), __uint_as_float(b)) : __uint_as_float(a) + __uint_as_float(b));
  };
  swap16(x[0], x[1]); swap16(x[2], x[3]);
  unsigned ab = op(x[0], x[1]), cd = op(x[2], x[3]);
  swap32(ab, cd);
  ab = __float_as_uint(fn(__uint_as_float(op(ab, cd)))); cd = ab;
  swap32(ab, cd);
  x[0] = x[1] = ab; x[2] = x[3] = cd;
  swap16(x[0], x[1]); swap16(x[2], x[3]);
}
DI int mod7(int v) { return v >= 7 ? v - 7 : v; }

template <int N1, int N2>
DI void gate_bar(unsigned p1, unsigned p2, unsigned lo, unsigned voff, rsrc_t rs) {
  unsigned p3, t, cnt;
  asm volatile(
      "s_waitcnt vmcnt(%9)\n\tv_sub_u32 %3, %0, %5\n\tv_cmp_lt_u32 vcc, 1, %3\n\ts_mov_b32 %4, %8\n\ts_cbranch_vccz 3f\n\t"
      "buffer_load_dword %0, %6, %7, 0 offen sc1\n\t"
      "s_waitcnt vmcnt(%10)\n\tv_sub_u32 %3, %1, %5\n\tv_cmp_lt_u32 vcc, 1, %3\n\ts_nop 0\n\ts_cbranch_vccz 2f\n\t"
      "buffer_load_dword %1, %6, %7, 0 offen sc1\n\ts_sleep 1\n\t"
      "buffer_load_dword %2, %6, %7, 0 offen sc1\n"
      "1:\n\t"
      "s_waitcnt vmcnt(2)\n\tv_sub_u32 %3, %0, %5\n\tv_cmp_lt_u32 vcc, 1, %3\n\ts_nop 0\n\ts_cbranch_vccz 2f\n\t"
      "buffer_load_dword %0, %6, %7, 0 offen sc1\n\t"
      "s_waitcnt vmcnt(2)\n\tv_sub_u32 %3, %1, %5\n\tv_cmp_lt_u32 vcc, 1, %3\n\ts_nop 0\n\ts_cbranch_vccz 2f\n\t"
      "buffer_load_dword %1, %6, %7, 0 offen sc1\n\t"
      "s_waitcnt vmcnt(2)\n\tv_sub_u32 %3, %2, %5\n\tv_cmp_lt_u32 vcc, 1, %3\n\ts_nop 0\n\ts_cbranch_vccz 2f\n\t"
      "buffer_load_dword %2, %6, %7, 0 offen sc1\n\t"
      "s_sub_u32 %4, %4, 1\n\ts_cbranch_scc0 1b\n"
      "2:\n\t"
      "s_barrier\n\ts_waitcnt vmcnt(0)\n\ts_branch 4f\n"
      "3:\n\t"
      "s_barrier\n\ts_waitcnt vmcnt(%11)\n"
      "4:"
      : "+v"(p1), "+v"(p2), "=&v"(p3), "=&v"(t), "=&s"(cnt)
      : "v"(lo), "v"(voff), "s"(rs), "i"(SPIN_CAP), "n"(N1 + 1), "n"(N2 + 1), "n"(N2) : "vcc", "scc", "memory");
}
DI void touch1(unsigned& sink, unsigned voff, rsrc_t rs) {
  asm volatile("buffer_load_dword %0, %1, %2, 0 offen" : "+v"(sink) : "v"(voff), "s"(rs) : "memory");
}
DI void settle2(v2u& a, v2u ref, unsigned va, rsrc_t rs) {
  unsigned cnt;
  asm volatile("s_mov_b32 %1, %5\n1:\n\tv_cmp_ne_u64 vcc, %0, %2\n\ts_sub_u32 %1, %1, 1\n\ts_cbranch_vccz 9f\n\ts_cbranch_scc1 9f\n\t"
               "buffer_load_dwordx2 %0, %3, %4, 0 offen sc1\n\ts_waitcnt vmcnt(0)\n\ts_branch 1b\n9:"
               : "+v"(a), "=&s"(cnt) : "v"(ref), "v"(va), "s"(rs), "i"(SPIN_CAP) : "vcc", "scc", "memory");
}

constexpr int CPRIO = 2;

enum { KX, KG, KQ, KH, KU };
struct Op { int kind, idx, batch, slot; };
constexpr int p2c(int j) { return (j + 6) % 7; }
constexpr Op OPS[] = {
    {KX, 0, -1, 10}, {KG, 0, -1, 12}, {KQ, 0, -1, 14}, {KX, 1, -1, 0}, {KG, 1, -1, 16},
    {KQ, 1, 0, 15}, {KX, 2, 0, 2}, {KG, 2, 0, 18}, {KQ, 2, 0, 9},
    {KX, 3, 1, 4}, {KG, 3, 1, 12}, {KQ, 3, 1, 14},
    {KX, 4, 2, 6}, {KG, 4, 2, 16}, {KQ, 4, 2, 15},
    {KX, 5, 3, 8}, {KG, 5, 3, 18},
    {KQ, 5, 4, 14}, {KX, 6, 4, 10}, {KG, 6, 4, 12},
    {KQ, 6, 5, 15}, {KH, 0, 5, -1}, {KH, 1, 6, -1}, {KH, 2, 7, -1}, {KH, 3, 7, -1}, {KH, 4, 8, -1},
    {KH, 5, 8, -1}, {KH, 6, 9, -1}, {KU, 0, 10, -1}, {KU, 1, 10, -1}, {KU, 2, 11, -1}, {KU, 3, 11, -1}, {KU, 4, 12, -1}, {KU, 5, 12, -1},
    {KU, 6, 13, -1}, {KU, 7, 13, -1}, {KU, 8, 14, -1}, {KU, 9, 14, -1}, {KU, 10, 16, -1}, {KU, 11, 16, -1},
    {KU, 12, 18, -1}, {KU, 13, 18, -1}, {KU, 14, 20, -1}, {KU, 15, 20, -1}, {KU, 16, 22, -1}, {KU, 17, 22, -1}};
constexpr int SYS[] = {0, 1, 2, 3, 4, 5, 6, 7, 8, 9, 10, 11, 12, 13, 14, 15, 16, 18, 20, 22, 24, 26, 28};
constexpr int NOPS = sizeof(OPS) / sizeof(Op), NSY = sizeof(SYS) / sizeof(int);
constexpr bool is_sync(int k) { for (int i = 0; i < NSY; ++i) if (SYS[i] == k) return true; return false; }
constexpr int sync_of(int st) { while (!is_sync(st)) --st; return st; }
constexpr int sync_after(int st) { ++st; while (st < 40 && !is_sync(st)) ++st; return st; }
constexpr int sync_before(int k) { --k; while (k >= 0 && !is_sync(k)) --k; return k; }
constexpr int op_units(int o) { return OPS[o].kind == KQ ? 1 : 2; }
constexpr int op_vm(int o) { return 2 * op_units(o); }
constexpr int op_need(int o) {
  return sync_of(OPS[o].kind == KH ? 7 + OPS[o].idx : OPS[o].kind == KU ? 14 + OPS[o].idx : OPS[o].idx);
}
constexpr int op_free(int o) {
  return sync_after(OPS[o].kind == KX && OPS[o].idx >= 1 && OPS[o].idx < 6 ? 8 + OPS[o].idx : op_need(o));
}
struct Alloc { int off[NOPS]; bool ok; };
constexpr Alloc make_alloc() {
  Alloc a{}; a.ok = true;
  int fr[20] = {};
  for (int i = 0; i < 20; ++i) fr[i] = -9;
  for (int o = 0; o < NOPS; ++o) {
    if (OPS[o].batch >= op_need(o)) a.ok = false;
    if (o > 0 && OPS[o].batch < OPS[o - 1].batch) a.ok = false;
    if (OPS[o].batch >= 0 && !is_sync(OPS[o].batch)) a.ok = false;
    int s = OPS[o].slot;
    if (s < 0) for (int i = 0; i < 20; i += 2) if (fr[i] <= OPS[o].batch && fr[i + 1] <= OPS[o].batch) { s = i; break; }
    if (s < 0) { a.ok = false; s = 0; }
    for (int i = 0; i < op_units(o); ++i) { if (fr[s + i] > OPS[o].batch) a.ok = false; fr[s + i] = op_free(o); }
    a.off[o] = s * 8192;
  }
  return a;
}
constexpr Alloc ALLOC = make_alloc();
static_assert(ALLOC.ok, "LDS schedule");
constexpr int find_op(int kind, int idx) { for (int o = 0; o < NOPS; ++o) if (OPS[o].kind == kind && OPS[o].idx == idx) return o; return -1; }
constexpr int lds_of(int kind, int idx) { return ALLOC.off[find_op(kind, idx)]; }
constexpr bool last_uses(int o) { for (int n = SYS[NSY - 1] - 14; n < 18; ++n) if (lds_of(KU, n) == o) return true; return false; }
constexpr int red_slot(int nth) {
  for (int q = 0; q < 10; ++q) if (!last_uses(q * 16384) && nth-- == 0) return q * 16384;
  return -1;
}
static_assert(red_slot(2) >= 0, "");
constexpr int first_op(int batch) { int o = 0; while (o < NOPS && OPS[o].batch < batch) ++o; return o; }
constexpr int vm_between(int from, int to) { int n = 0; for (int o = from; o < to; ++o) n += op_vm(o); return n; }
constexpr int loader_wait(int k) {
  int last = -1;
  for (int o = 0; o < NOPS; ++o) if (op_need(o) <= k) last = o;
  const int n = vm_between(last + 1, first_op(k)), room = 60 - vm_between(first_op(k), first_op(k + 1));
  return n < room ? n : room;
}
constexpr int KPQ = 9, KP1 = 10, KP2 = 16, KG1 = 15, KG2 = 22, KG3 = 26;
static_assert(KPQ >= 8 && KP1 >= 8 && is_sync(KG1) && is_sync(KG2) && is_sync(KG3) && is_sync(KP1) && is_sync(KP2) && is_sync(KPQ), "");
static_assert(sync_before(sync_before(KG2)) >= KG1 && sync_before(sync_before(KG3)) >= KG2, "one gate at a time");
constexpr int lout(int P) {
  if (P < 0) return 0;
  const int cap = P < KG2 - 15 ? 7 : P < KG3 - 15 ? 14 : 18, n = P < KG2 - 15 ? 7 : 10 + P - (KG2 - 15);
  return n < cap ? n : cap;
}

__global__ __launch_bounds__(768, 3) __attribute__((flatten)) void fused_kernel(Params p) {
  __shared__ __attribute__((aligned(16))) char smem[163840];
  const unsigned lds0 = (unsigned)(size_t)(LAS char*)smem;
  const int tid = threadIdx.x, lane = tid & 63, w = __builtin_amdgcn_readfirstlane(tid >> 6);
  const int b = blockIdx.x, xcd = b & 7, bi = b >> 3;
  unsigned long long pc, q;
  asm volatile("s_load_dwordx2 %0, %1, %2" : "=s"(q) : "s"(__builtin_amdgcn_kernarg_segment_ptr()), "s"((bi & 7) == 6 ? 64 : (bi & 7) * 8));
  asm volatile("s_getpc_b64 %0" : "=s"(pc));
  const bool isF = bi < 28;
  const int lin = bi + (xcd >> 1) * (isF ? 28 : 4) + (isF ? 0 : 84);
  const int s = lin >> 3, r = (xcd & 1) * 8 + (lin & 7), a = s - 14;
  const int c0 = mod7((((r & 7) + 2 * s) % 3) * 2 + (xcd == 7 ? 3 : xcd));
  constexpr int ASLOT = 51200, AGT = 3 * ASLOT;
#define TILE_VALUES                                                                                                  \
  const int bi_ = sfresh(bi), t2 = bi_ >= 14 ? bi_ - 14 : bi_, r2 = 2 * xcd + (bi_ >= 14 ? 1 : 0);                   \
  const int n2 = t2 >= 7 ? t2 - 7 : t2, rb2 = 2 * r2 + (t2 >= 7 ? 1 : 0);                                            \
  const int rot = sel(xcd == 7, 0, 2 * xcd);                                                                           \
  auto p2unit = [&](int n, int e) -> int {            \
    if (n < 14) { const int v = 2 * (n % 7) + e + rot; return 2 * sel(v >= 14, v - 14, v) + n / 7; }                 \
    return 28 + ((2 * n + e + xcd) & 7);                                                                             \
  }

  if (__builtin_expect(w < 4, 1)) {
    const int l8 = lane >> 3, c8 = lane & 7, e = w >> 1, hw = w & 1;
    const rsrc_t rx = mkrs(p.x, S_ * D_ * 2);
    unsigned vx[4], vq[4];
    const unsigned m0w = lds0 + w * 4096;
    for (int i = 0; i < 4; ++i) vx[i] = (8 * i + l8) * XS + ((c8 ^ (4 * (i & 1) + (l8 >> 1))) << 4) - 1024 * i;
    unsigned sink;
    {
      const int k = bi & 7;
      const unsigned step = sel(k == 6, (WS_BYTES - 4) / 3, sel(k == 5, (D_ * XS - 4) / 3, (D_ * QS - 4) / 3)) & ~3u;
      asm volatile("s_waitcnt lgkmcnt(0)\n\tglobal_load_dword %0, %1, %2 sc1" : "=v"(sink) : "v"(w * step), "s"(q) : "memory");
    }
    dma4(m0w + (isF ? ALLOC.off[0] : 0), vx, rx, (r * 128 + 32 * w) * XS + c0 * 128);
    SB;
    const rsrc_t rq = mkrs(p.wqkv, D_ * 3 * D_ * 2), rw = mkrs(p.ws, WS_BYTES);
    for (int i = 0; i < 4; ++i) vq[i] = (8 * i + l8) * QS + ((c8 ^ (l8 & 2) ^ ((i & 1) << 2)) << 4) - 1024 * i;
    auto pull_late = [&]() {
      const int per = sel(w == 0, 56, sel(w == 1, 14, 0));
      touch1(sink, sel(lane < per, ((xcd * 28 + bi) * per + lane) * 128u, OOB), mkrs(sel(w == 0, p.wd, p.wo), sel(w == 0, F_ * XS, D_ * XS)));
    };
    const bool pw = w == 0;
    const unsigned reqp0 = REQ_OFF + (r * 16 + s) * 256 + lane * 8;
    if (__builtin_expect(isF, 1)) {
      unsigned vg[4], vd[4];
      for (int i = 0; i < 4; ++i) vg[i] = vq[i] + (8 * i + l8) * (GS - QS);
      const rsrc_t rg = mkrs(e ? p.wu : p.wg, D_ * F_ * 2);
      static_assert(OPS[1].kind == KG && OPS[2].kind == KQ, "");
      dma4(m0w + ALLOC.off[1], vg, rg, (c0 * 64 + 32 * hw) * GS + s * 256);
      dma2(lds0 + ALLOC.off[2] + w * 2048, vq[0], vq[1], rq, (c0 * 64 + 16 * w) * QS + s * 128);
      SB;
      TILE_VALUES;
      for (int i = 0; i < 4; ++i) vd[i] = vq[i] - (8 * i + l8) * (QS - XS);
      const rsrc_t rd = mkrs(p.wd, F_ * D_ * 2), ro = mkrs(p.wo, D_ * D_ * 2);
      auto issue = [&](auto oc) {
        constexpr int O = decltype(oc)::value, K = OPS[O].kind, I = OPS[O].idx, off = ALLOC.off[O];
        if constexpr (K == KU) {
          const int v = p2unit(I, e);
          dma4(m0w + off, vd, I < 14 ? rd : ro, (I < 14 ? 64 * v + 32 * hw : 28 * (2 * (v - 28) + hw)) * XS + n2 * 128);
        } else {
          const int c = mod7((K == KH ? p2c(I) : I) + c0);
          if constexpr (K == KX) dma4(m0w + off, vx, rx, (r * 128 + 32 * w) * XS + c * 128);
          else if constexpr (K == KQ) dma2(lds0 + off + w * 2048, vq[0], vq[1], rq, (c * 64 + 16 * w) * QS + s * 128);
          else dma4(m0w + off, vg, rg, (c * 64 + 32 * hw) * GS + s * 256 + (K == KH ? 128 : 0));
        }
      };
      const int u = s >= 7 ? s - 7 : s, isk = s >= 7, aa = lane & 1, nxt = lane >= 16;
      const bool inr = u >= 3 * aa && u <= 3 * aa + 3;
      const bool pvalid = pw && (lane < 14 || (lane < 16 && inr) || (lane < 18 && inr && isk && r < 15));
      const unsigned ackc = sel(pw && lane < 16, ACK_OFF + (r2 * 14 + t2) * 128 + lane * 8, OOB);
      const unsigned reqc = sel(pw && lane < 16, REQ_OFF + (r2 * 16 + lane) * 256 + t2 * 8, OOB);
      const unsigned ackp = sel(pvalid, sel(lane < 14, ACK_OFF + (r * 14 + lane) * 128 + s * 8,
                                            ACK_OFF + (224 + 2 * (r + nxt) + aa) * 128 + (sel(nxt, 8, isk ? 4 : 0) + u - 3 * aa) * 8), OOB);
      const unsigned reqp = sel(pvalid, reqp0, OOB);
      v2u a0 = ld64(ackc, rw), pak = ld64(ackp, rw), poll;
      sfor<3, first_op(0)>(issue);
      unsigned p1, p2;
      const bool ish = pw && lane < 14, iso = pw && (lane == 14 || lane == 15);
      const unsigned vh_ = sel(ish, ackc, OOB), vo_ = sel(iso, ackc, OOB);
      sfor<0, NSY>([&](auto ic) {
        constexpr int K = SYS[decltype(ic)::value], K1 = sync_before(K), N1 = sync_after(K), N2 = sync_after(N1);
        if constexpr (K == 8) {
          wait_for<vm_between(first_op(6), first_op(8))>(poll);
          settle2(poll, v2u{sel(pvalid, pak.x ^ KX0, 0u), sel(pvalid, pak.y ^ KX1, 0u)}, reqp, rw);
        }
        if constexpr (K == KG1 || K == KG2 || K == KG3) {
          asm volatile("s_waitcnt vmcnt(%0)" ::"i"(loader_wait(K)) : "memory");
          gate_bar<vm_between(first_op(K1), first_op(K)), vm_between(first_op(K1), first_op(K))>(
              p1, p2, sel(K == KG3 ? iso : ish, a0.x + (K == KG2 ? 2u : 1u), 0u), K == KG3 ? vo_ : vh_, rw);
        } else sync<loader_wait(K) + (K == 0 ? 2 : 0)>();
        if constexpr (K == 1) { landed(a0); landed(pak); st64(v2u{a0.x ^ KX0, a0.y ^ KX1}, reqc, rw); }
        if constexpr (K == KPQ) st64(v2u{pak.x + 1u, pak.y}, sel(lane >= 14, ackp, OOB), rw);
        if constexpr (K == KP1) st64(v2u{pak.x + 1u, pak.y}, sel(lane < 14, ackp, OOB), rw);
        if constexpr (K == KP2) st64(v2u{pak.x + 2u, pak.y}, sel(lane < 14, ackp, OOB), rw);
        if constexpr (K == 6) poll = ld64(reqp, rw);
        if constexpr (N1 == KG1 || N1 == KG2 || N1 == KG3) p2 = ld32(N1 == KG3 ? vo_ : vh_, rw, 0);
        sfor<first_op(K), first_op(K + 1)>(issue);
        if constexpr (N2 == KG1 || N2 == KG2 || N2 == KG3) p1 = ld32(N2 == KG3 ? vo_ : vh_, rw, 0);
        if constexpr (K == 6) pull_late();
        if constexpr (K == 10) landed(sink);
      });
    } else {
      const unsigned vh = (8 * hw + l8) * XS + ((c8 ^ (4 * hw + (l8 >> 1))) << 4);
      auto part = [&](auto ic, auto jc) {
        constexpr int I = decltype(ic)::value, J = decltype(jc)::value;
        const int c = mod7(I + c0);
        const unsigned m0 = m0w + (I % 3) * ASLOT;
        if constexpr (J == 0) dma4(m0, vx, rx, (r * 128 + 32 * w) * XS + c * 128);
        else if constexpr (J < 3) dma4(m0 + J * 16384, vq, rq, (c * 64 + 32 * hw) * QS + 1792 + (3 * a + 2 * (J - 1) + e) * 128);
        else dma1(lds0 + (I % 3) * ASLOT + 49152 + hw * 1024, vh, rx, r > 0 ? (r * 128 - 16) * XS + c * 128 : OOB);
      };
      sfor<1, 4>([&](auto j) { part(IC(0), j); });
      dma1(lds0 + AGT, lane * 16, mkrs(p.gw, D_ * 2), 0);
      const bool cvalid = pw && (lane < 8 || (lane < 12 && r > 0)), pvalid = pw && lane < 14;
      const int ps = sel(lane < 4, 3 * a + lane, 7 + 3 * a + (lane & 3)), pr = sel(lane < 8, r, r - 1);
      const unsigned ackc = sel(cvalid, ACK_OFF + (224 + 2 * r + a) * 128 + lane * 8, OOB);
      const unsigned reqc = sel(cvalid, REQ_OFF + (pr * 16 + ps) * 256 + (sel(lane < 8, 14, 16) + a) * 8, OOB);
      const unsigned ackp = sel(pvalid, ACK_OFF + (r * 14 + lane) * 128 + s * 8, OOB), reqp = sel(pvalid, reqp0, OOB);
      v2u a0 = ld64(ackc, rw), pak = ld64(ackp, rw), poll;
      part(IC(1), IC(0));
      sfor<0, 7>([&](auto kc) {
        constexpr int K = decltype(kc)::value;
        sync<(K == 0 ? 4 : K < 6 ? 13 : 0) + (K == 1 || K == 3 ? 1 : K == 2 ? 2 : 0)>();
        if constexpr (K == 0) sfor<1, 4>([&](auto j) { part(IC(1), j); });
        if constexpr (K == 0) { landed(a0); landed(pak); st64(v2u{a0.x ^ KX0, a0.y ^ KX1}, reqc, rw); }
        if constexpr (K == 4) poll = ld64(reqp, rw);
        if constexpr (K + 2 < 7) sfor<0, 4>([&](auto j) { part(IC(K + 2), j); });
        if constexpr (K < 2)
          touch1(sink, sel(lane < 49, ((((bi - 28) * 8 + xcd) * 4 + w) * 49 + lane) * 256u + 128u, OOB), mkrs(K ? p.wu : p.wg, D_ * GS));
      });
      landed(sink);
      landed(poll);
      settle2(poll, v2u{sel(pvalid, pak.x ^ KX0, 0u), sel(pvalid, pak.y ^ KX1, 0u)}, reqp, rw);
      const unsigned p1 = ld32(ackc, rw, 0);
      __builtin_amdgcn_s_sleep(3);
      const unsigned p2 = ld32(ackc, rw, 0);
      __builtin_amdgcn_s_sleep(3);
      gate_bar<0, 0>(p1, p2, sel(cvalid, a0.x + 1u, 0u), ackc, rw);
      bar();
      st64(v2u{pak.x + 1u, pak.y}, ackp, rw);
    }
  } else if (isF) {
    __builtin_amdgcn_s_setprio(CPRIO);
    TILE_VALUES;
    asm volatile("s_waitcnt lgkmcnt(0)" : "+s"(q));
    unsigned pfd = fetch_code(bi * 8 + w - 4, pc);
    const int cw = w - 4, grp = lane >> 4, m16 = lane & 15, q4 = m16 >> 2, p4 = lane & 3;
    const rsrc_t rw = mkrs(p.ws, WS_BYTES);
    const int rp = cw >> 2, cb = cw & 3;
    const unsigned va2 = lane * 16;
    const unsigned f1 = (q4 >> 1) | ((grp & 1) << 1), rowp = lds0 + (8 * grp + q4) * 128 + p4 * 8;
    unsigned bx[2][3], bw[3];
    {
      const unsigned xr = lds0 + rp * 8192 + m16 * 128;
      seg3(bx[0], xr + ((grp ^ (m16 >> 1)) << 4));
      seg3(bx[1], xr + (((4 + grp) ^ (m16 >> 1)) << 4));
      seg3(bw, rowp + ((cb ^ f1) << 5));
    }
    s16x8 xk[2][4], xf[2][4], wf[2][3];
    f32x4 a1[3][4], a2[2][4];
    sfor<0, 4>([&](auto i) { a1[0][i] = a1[1][i] = a1[2][i] = a2[0][i] = a2[1][i] = f32x4{0, 0, 0, 0}; });
    v4u al[18][2];
    auto rd1 = [&](auto tc, auto jc) {
      constexpr int T = decltype(tc)::value, J = decltype(jc)::value, st = (T % 14) / 2, kk = T & 1;
      if constexpr (T < 28) {
        if constexpr (J < 2) wf[kk][J] = tr2<lds_of(T < 14 ? KG : KH, st) + J * 8192 + kk * 4096, 512>(bw);
        else if constexpr (J == 2) { if constexpr (T < 14) wf[kk][2] = tr2<lds_of(KQ, st) + kk * 4096, 512>(bw); }
        else if constexpr (T < 2) xk[T][J - 3] = rd128<lds_of(KX, 0) + (J - 3) * 2048>(bx[kk]);
        else if constexpr (T < 14 || T >= 18) xf[kk][J - 3] = rd128<lds_of(KX, T < 14 ? st : p2c(st)) + (J - 3) * 2048>(bx[kk]);
      }
    };
    auto xop = [&](auto tc, auto ic) -> s16x8 {
      constexpr int T = decltype(tc)::value, I = decltype(ic)::value;
      if constexpr (T < 2 || T == 16 || T == 17) return xk[T & 1][I]; else return xf[T & 1][I];
    };
    auto issue_load = [&](auto lc) {
      constexpr int L = decltype(lc)::value;
      const int v = p2unit(L, cb >> 1);
      const unsigned so = A2_OFF + (rb2 * 4 + rp * 2) * A2_RT + (2 * v + (cb & 1)) * 1024;
      al[L][0] = ld128(va2, rw, so);
      al[L][1] = ld128(va2, rw, so + A2_RT);
    };
    constexpr int ORD[7] = {0, 3, 1, 2, 4, 5, 6};
    auto step1 = [&](auto tc) {
      constexpr int T = decltype(tc)::value, kk = T & 1;
      sfor<0, 4>([&](auto i) {
        constexpr int I = decltype(i)::value;
        if constexpr (T < 14) {
          a1[0][i] = mfma16x32(wf[kk][0], xop(tc, i), a1[0][i]); SB;
          rd1(IC(T + 1), IC(ORD[2 * I])); SB;
          a1[1][i] = mfma16x32(wf[kk][1], xop(tc, i), a1[1][i]); SB;
          if constexpr (2 * I + 1 < 7) { rd1(IC(T + 1), IC(ORD[(2 * I + 1) % 7])); SB; }
          a1[2][i] = mfma16x32(wf[kk][2], xop(tc, i), a1[2][i]); SB;
        } else {
          a2[0][i] = mfma16x32(wf[kk][0], xop(tc, i), a2[0][i]); SB;
          rd1(IC(T + 1), IC(ORD[2 * I])); SB;
          a2[1][i] = mfma16x32(wf[kk][1], xop(tc, i), a2[1][i]); SB;
          if constexpr (2 * I + 1 < 7) { rd1(IC(T + 1), IC(ORD[(2 * I + 1) % 7])); SB; }
        }
      });
    };
    const unsigned vh = (grp & 1) * A2_RT + (grp >> 1) * 256 + m16 * 16;
    auto store_h = [&](const f32x4 (&g)[4], const f32x4 (&u)[4], int pair) {
      sfor<0, 2>([&](auto e) {
        float h[8];
        sfor<0, 8>([&](auto n) { h[n] = sigm(g[2 * e + n / 4][n % 4]) * u[2 * e + n / 4][n % 4]; });
        unsigned a0 = pk(h[0], h[1]), a1_ = pk(h[2], h[3]), b0 = pk(h[4], h[5]), b1 = pk(h[6], h[7]);
        swap16(a0, b0); swap16(a1_, b1);
        st128<true>(v4u{a0, a1_, b0, b1}, vh, rw, A2_OFF + (r * 8 + 4 * rp + 2 * e) * A2_RT + (4 * s + 2 * pair) * 1024 + cb * 512);
      });
    };
    bar();
    sfor<0, 7>([&](auto j) { rd1(IC(0), IC(ORD[j])); });
    SB;
    sfor<0, 14>([&](auto ic) {
      constexpr int I = decltype(ic)::value, K = I + 1;
      if constexpr (I == 2) wait_for<0>(pfd);
      if constexpr (I == 7) {
        sfor<0, 2>([&](auto e) {
          const f32x4 &c0_ = a1[2][2 * e], &c1 = a1[2][2 * e + 1];
          st128<true>(v4u{pk(c0_[0], c0_[1]), pk(c0_[2], c0_[3]), pk(c1[0], c1[1]), pk(c1[2], c1[3])}, va2, rw,
                      QK_OFF + (r * 4 + 2 * rp + e) * QK_RP + (16 * s + 4 * cb) * 256);
        });
        store_h(a1[0], a1[1], 0);
        SB;
      }
      step1(IC(2 * I));
      if constexpr (is_sync(K)) {
        sync<(K == KP1 ? 0 : K == KPQ ? 2 : 63)>();
        SB;
      }
      step1(IC(2 * I + 1));
    });
    store_h(a2[0], a2[1], 1);

    unsigned b2[4][3];
    sfor<0, 4>([&](auto t) { seg3(b2[t], rowp + cb * 4096 + ((((t + cb) & 3) ^ f1) << 5)); });
    f32x4 oh[2][4];
    sfor<0, 4>([&](auto t) { oh[0][t] = oh[1][t] = f32x4{0, 0, 0, 0}; });
    LAS char* rbase = (LAS char*)smem + rp * 8192 + lane * 16;
    auto send = [&](auto t) {
      constexpr int o = red_slot(t - 1);
      LAS char* d = rbase + o + ((t + cb) & 3) * 2048;
      *(LAS f32x4*)d = oh[0][t];
      *(LAS f32x4*)(d + 1024) = oh[1][t];
    };
    s16x8 wv[2][4];
    unsigned gb0, gb1;
    auto rd2 = [&](auto pc, auto t) {
      constexpr int P = decltype(pc)::value;
      if constexpr (P < 18) wv[P & 1][t] = tr2<lds_of(KU, P), 512>(b2[t]);
    };
    static_assert(KG1 == 15 && KP2 > 14, "");
    sfor<0, 4>([&](auto t) { rd2(IC(0), t); });
    SB;
    sfor<0, 18>([&](auto pc) {
      constexpr int P = decltype(pc)::value, K = 15 + P;
      if constexpr (is_sync(K)) {
        sync<(K == KP2 ? 2 * lout(P - 1) : 63)>();
        if constexpr (K == KG3) {
          gb0 = ld32(m16 * 4, rw, G_OFF + (rb2 * 64 + rp * 32) * 4);
          gb1 = ld32(m16 * 4, rw, G_OFF + (rb2 * 64 + rp * 32 + 16) * 4);
        }
      }
      sfor<lout(P - 1), lout(P)>(issue_load);
      SB;
      static_assert(lout(P) > P, "load order");
      wait_for<2 * (lout(P) - 1 - P)>(al[P][1]);
      landed(al[P][0]);
      const s16x8 av0 = __builtin_bit_cast(s16x8, al[P][0]), av1 = __builtin_bit_cast(s16x8, al[P][1]);
      if constexpr (P == 14) {
        landed(gb0); landed(gb1);
        const float g0 = __uint_as_float(gb0), g1 = __uint_as_float(gb1);
        sfor<0, 4>([&](auto t) { oh[0][t] *= g0; oh[1][t] *= g1; });
        SB;
      }
      sfor<0, 4>([&](auto tt) {
        constexpr int t = P == 17 ? (tt + 1) & 3 : tt;
        oh[0][t] = mfma16x32(wv[P & 1][t], av0, oh[0][t]); oh[1][t] = mfma16x32(wv[P & 1][t], av1, oh[1][t]);
        SB;
        rd2(IC(P + 1), IC(P == 16 ? (tt + 1) & 3 : tt));
        if constexpr (P == 17 && tt > 0) send(tt);
        SB;
      });
    });
    sync_lds();
    f32x4 e0 = oh[0][0], e1 = oh[1][0];
    sfor<1, 4>([&](auto t) {
      constexpr int o = red_slot(t - 1);
      e0 += *(const LAS f32x4*)(rbase + o + cb * 2048);
      e1 += *(const LAS f32x4*)(rbase + o + cb * 2048 + 1024);
    });
    unsigned a0 = pk(e0[0], e0[1]), a1_ = pk(e0[2], e0[3]), b0 = pk(e1[0], e1[1]), b1 = pk(e1[2], e1[3]);
    swap16(a0, b0); swap16(a1_, b1);
    st128<false>(v4u{a0, a1_, b0, b1}, ((grp & 1) * 16 + m16) * XS + (grp >> 1) * 16, mkrs(p.out, S_ * D_ * 2),
                 (rb2 * 64 + rp * 32) * XS + n2 * 128 + cb * 32);
  } else {
    asm volatile("s_waitcnt lgkmcnt(0)" : "+s"(q));
    unsigned pfd = fetch_code(bi * 8 + w - 4, pc);
    __builtin_amdgcn_s_setprio(CPRIO);
    const int cw = w - 4, grp = lane >> 4, m16 = lane & 15, q4 = m16 >> 2, p4 = lane & 3;
    const rsrc_t rw = mkrs(p.ws, WS_BYTES);
    unsigned bv0[3], bv1[3], bx0[3], bx1[3];
    {
      const unsigned f1 = (q4 >> 1) | ((grp & 1) << 1);
      const unsigned rowp = lds0 + 16384 + (8 * grp + q4) * 128;
      const int lc0 = 28 * cw + 32 * a + 4 * p4, lc1 = 28 * cw + 32 * a + 16 + (p4 == 3 ? 8 : 4 * p4);
      seg3(bv0, rowp + (lc0 >> 6) * 8192 + ((((lc0 >> 4) & 3) ^ f1) << 5) + (lc0 & 15) * 2);
      seg3(bv1, rowp + (lc1 >> 6) * 8192 + ((((lc1 >> 4) & 3) ^ f1) << 5) + (lc1 & 15) * 2);
      const unsigned xr0 = lds0 + m16 * 128;
      seg3(bx0, xr0 + ((grp ^ (m16 >> 1)) << 4));
      seg3(bx1, xr0 + (((4 + grp) ^ (m16 >> 1)) << 4));
    }
    constexpr int XR = 5;
    s16x8 xf[XR], vf[2][2], gf[2];
    f32x4 av[9][2], ag = {0, 0, 0, 0};
    sfor<0, 9>([&](auto j) { av[j][0] = f32x4{0, 0, 0, 0}; av[j][1] = f32x4{0, 0, 0, 0}; });
    auto rdx = [&](auto nc) {
      constexpr int N = decltype(nc)::value, T = N / 10, J = N % 10, kk = T & 1, so = ((T / 2) % 3) * ASLOT;
      if constexpr (T < 14) {
        if constexpr (J == 0) xf[N % XR] = kk ? rd128<so + 49152>(bx1) : rd128<so + 49152>(bx0);
        else if constexpr (J < 9) xf[N % XR] = kk ? rd128<so + (J - 1) * 2048>(bx1) : rd128<so + (J - 1) * 2048>(bx0);
        else xf[N % XR] = *(const LAS s16x8*)(size_t)((kk ? bx1[so >> 16] : bx0[so >> 16]) + cw * 2048 + (so & 0xFFFF));
      }
    };
    auto rdw = [&](auto tc) {
      constexpr int T = decltype(tc)::value, kk = T & 1, so = ((T / 2) % 3) * ASLOT;
      if constexpr (T < 14) {
        vf[kk][0] = tr2<so + kk * 4096, 512>(bv0);
        vf[kk][1] = tr2<so + kk * 4096, 512>(bv1);
        gf[kk] = *(const LAS s16x8*)(size_t)(lds0 + AGT + mod7(T / 2 + c0) * 128 + kk * 64 + grp * 16);
      }
    };
    bar();
    rdw(IC(0));
    sfor<0, XR - 1>(rdx);
    SB;
    sfor<0, 140>([&](auto nc) {
      constexpr int N = decltype(nc)::value, T = N / 10, J = N % 10, kk = T & 1;
      if constexpr ((N + XR - 1) % 20 == 0 && N + XR - 1 < 140) { sync_lds(); SB; }
      if constexpr (N == 30) wait_for<0>(pfd);
      if constexpr ((N + XR - 1) % 10 == 0) rdw(IC(T + 1));
      rdx(IC(N + XR - 1));
      SB;
      if constexpr (J < 9) {
        av[J][0] = mfma16x32(xf[N % XR], vf[kk][0], av[J][0]);
        av[J][1] = mfma16x32(xf[N % XR], vf[kk][1], av[J][1]);
      } else ag = mfma16x32(gf[kk], xf[N % XR], ag);
      SB;
    });
    const float gme = sigm(ag[0]);
    unsigned gsig = __float_as_uint(1.0f - gme);
    *(LAS float*)(size_t)(lds0 + AGT + 1024 + cw * 64 + m16 * 4) = gme;
    SB;
    sync_lds();
    const int hd = 8 * a + cw;
    const unsigned vlo = lane * 16, vhi = sel(grp < 3, 1024u + lane * 16, OOB);
    v4u ql[5], qh[5], kl[5], kh[5];
    auto ldqk = [&](auto pc) {
      constexpr int P = decltype(pc)::value;
      const unsigned base = QK_OFF + (r * 4 + P - 1) * QK_RP + 7 * hd * 256;
      const unsigned kb = (P == 0 && r == 0) ? OOB : base + 112 * 256;
      kl[P] = ld128(vlo, rw, kb);
      kh[P] = ld128((P == 0 && r == 0) ? OOB : vhi, rw, (P == 0 && r == 0) ? 0u : kb);
      if constexpr (P > 0) { ql[P] = ld128(vlo, rw, base); qh[P] = ld128(vhi, rw, base); }
    };
    sfor<0, 3>(ldqk);
    SB;
    v4u vp[5][2];
    sfor<0, 9>([&](auto j) {
      vp[j / 2][0][2 * (j & 1)] = pk(av[j][0][0], av[j][0][1]); vp[j / 2][0][2 * (j & 1) + 1] = pk(av[j][0][2], av[j][0][3]);
      vp[j / 2][1][2 * (j & 1)] = sel(m16 < 12, pk(av[j][1][0], av[j][1][1]), 0u);
      vp[j / 2][1][2 * (j & 1) + 1] = sel(m16 < 12, pk(av[j][1][2], av[j][1][3]), 0u);
    });
    vp[4][0][2] = vp[4][0][3] = vp[4][1][2] = vp[4][1][3] = 0u;
    const int par = lane & 1;
    const bool curA = 4 * grp + par <= m16, curB = 4 * grp + 2 + par <= m16;
    constexpr float NEG = -3.0e38f, SCALE = 0.18898223650461361f;
    f32x4 bc = {sel(!par && curA, 0.f, NEG), sel(par && curA, 0.f, NEG), sel(!par && curB, 0.f, NEG), sel(par && curB, 0.f, NEG)};
    f32x4 bp = {sel(!par && !curA, 0.f, NEG), sel(par && !curA, 0.f, NEG), sel(!par && !curB, 0.f, NEG), sel(par && !curB, 0.f, NEG)};
    f32x4 bp0 = bp + sel(r == 0, -1e9f / SCALE, 0.f);
    const unsigned sa = par ? 0x01000c0cu : 0x0c0c0100u, sb = par ? 0x03020c0cu : 0x0c0c0302u;
    unsigned spa = sel(curA, 0x0c0c0c0cu, sa), sca = sel(curA, sa, 0x0c0c0c0cu), spb = sel(curB, 0x0c0c0c0cu, sb), scb = sel(curB, sb, 0x0c0c0c0cu);
    landed(gsig); landed(bc); landed(bp); landed(bp0); landed(spa); landed(sca); landed(spb); landed(scb);
    sfor<0, 5>([&](auto j) { landed(vp[j][0]); landed(vp[j][1]); });
    SB;
    const float gq[2] = {*(const LAS float*)(size_t)(lds0 + AGT + 1024 + lane * 4), *(const LAS float*)(size_t)(lds0 + AGT + 1280 + lane * 4)};
    const unsigned vo = ((grp & 1) * 2 + (grp >> 1)) * 256 + m16 * 16;
    auto frag = [&](const v4u& lo, const v4u& hi, int e) { return __builtin_bit_cast(s16x8, e ? hi : lo); };
    SB;
    sfor<0, 2>([&](auto hc) {
      constexpr int H = decltype(hc)::value;
      wait_for<(H == 0 ? 0 : 4)>(qh[2 * H + 2]);
      if constexpr (H == 0) sfor<3, 5>(ldqk);
      sfor<2 * H + 1, 2 * H + 3>([&](auto P) { landed(kl[P]); landed(kh[P]); landed(ql[P]); landed(qh[P]); });
      if constexpr (H == 0) { landed(kl[0]); landed(kh[0]); regroup(kl[0], kh[0]); }
      sfor<2 * H + 1, 2 * H + 3>([&](auto P) { regroup(kl[P], kh[P]); regroup(ql[P], qh[P]); });
      SB;
      asm volatile("s_nop 1");
      SB;
      f32x4 sp[4], su[4];
      sfor<0, 4>([&](auto j) {
        constexpr int I = 4 * H + j, P = 1 + I / 2, e = I & 1, PP = e ? P : P - 1, ep = e ^ 1;
        const s16x8 q = frag(ql[P], qh[P], e);
        sp[j] = mfma16x32(frag(kl[PP], kh[PP], ep), q, I == 0 ? bp0 : bp);
        su[j] = mfma16x32(frag(kl[P], kh[P], e), q, bc);
      });
      SB;
      asm volatile("s_nop 7");
      SB;
      float la[4], lb[4]; unsigned x[4];
      sfor<0, 4>([&](auto j) { la[j] = vmax3(su[j][0], su[j][1], sp[j][0]); lb[j] = vmax3(su[j][2], su[j][3], sp[j][2]); });
      SB;
      sfor<0, 4>([&](auto j) { la[j] = vmax(la[j], sp[j][1]); lb[j] = vmax(lb[j], sp[j][3]); });
      SB;
      sfor<0, 4>([&](auto j) { x[j] = __float_as_uint(vmax_n(la[j], lb[j])); });
      quad_reduce<true>(x, [&](float m) { return m * -(SCALE * LOG2E); });
      sfor<0, 4>([&](auto j) {
        la[j] = __builtin_amdgcn_exp2f(__builtin_fmaf(la[j], SCALE * LOG2E, __uint_as_float(x[j])));
        lb[j] = __builtin_amdgcn_exp2f(__builtin_fmaf(lb[j], SCALE * LOG2E, __uint_as_float(x[j])));
      });
      SB;
      sfor<0, 4>([&](auto j) { x[j] = __float_as_uint(la[j] + lb[j]); });
      quad_reduce<false>(x, [&](float t) { return __builtin_amdgcn_rcpf(t) * gq[H]; });
      v4u pr_[4];
      sfor<0, 4>([&](auto j) {
        const unsigned t = pk(la[j] * __uint_as_float(x[j]), lb[j] * __uint_as_float(x[j]));
        pr_[j] = v4u{__builtin_amdgcn_perm(t, t, spa), __builtin_amdgcn_perm(t, t, spb), __builtin_amdgcn_perm(t, t, sca), __builtin_amdgcn_perm(t, t, scb)};
      });
      SB;
      f32x4 o_[4][2];
      sfor<0, 4>([&](auto j) {
        constexpr int I = 4 * H + j;
        sfor<0, 2>([&](auto t) {
          if constexpr ((I & 1) == 0) o_[j][t] = mfma16x32(__builtin_bit_cast(s16x8, vp[I / 2][t]), __builtin_bit_cast(s16x8, pr_[j]), f32x4{0, 0, 0, 0});
          else o_[j][t] = mfma16x16(v2u{vp[I / 2][t][2], vp[I / 2][t][3]}, v2u{pr_[j][0], pr_[j][1]}, f32x4{0, 0, 0, 0});
        });
      });
      SB;
      sfor<0, 4>([&](auto j) {
        constexpr int I = 4 * H + j;
        if constexpr (I & 1) sfor<0, 2>([&](auto t) { o_[j][t] = mfma16x16(v2u{vp[I / 2 + 1][t][0], vp[I / 2 + 1][t][1]}, v2u{pr_[j][2], pr_[j][3]}, o_[j][t]); });
      });
      SB;
      sfor<0, 4>([&](auto jj) {
        constexpr int j = (jj & 1) * 2 + jj / 2;
        unsigned a0 = pk(o_[j][0][0], o_[j][0][1]), a1 = pk(o_[j][0][2], o_[j][0][3]);
        unsigned b0 = pk(o_[j][1][0], o_[j][1][1]), b1 = pk(o_[j][1][2], o_[j][1][3]);
        swap16(a0, b0); swap16(a1, b1);
        st128<true>(v4u{a0, a1, b0, b1}, vo, rw, A2_OFF + (r * 8 + 4 * H + j) * A2_RT + (56 + hd) * 1024);
      });
      SB;
    });
    __builtin_amdgcn_raw_buffer_store_b32(gsig, rw, (a == 0 && lane < 16) ? lane * 4 : OOB, G_OFF + (r * 128 + cw * 16) * 4, SC1);
    sync<0>();
  }
}

extern "C" void kernel_launch(void* const* d_in, const int* in_sizes, int n_in, void* d_out, int out_size, void* d_ws, size_t ws_size,
                              hipStream_t stream) {
  Params p;
  p.x = (const char*)d_in[0]; p.wqkv = (const char*)d_in[1]; p.wo = (const char*)d_in[2];
  p.wg = (const char*)d_in[4]; p.wu = (const char*)d_in[5]; p.wd = (const char*)d_in[6]; p.gw = (const char*)d_in[7];
  p.out = (char*)d_out; p.ws = (char*)d_ws;
  fused_kernel<<<256, 768, 0, stream>>>(p);
}
```
